```python
import torch

def _softmax(x, axis=-1):
    return torch.softmax(x, dim=axis)
def _gelu(x):
    return torch.nn.functional.gelu(x, approximate="tanh")
def _silu(x):
    return torch.nn.functional.silu(x)
def _sigmoid(x):
    return torch.sigmoid(x)
def _layernorm(x, axis=-1, eps=1e-5):
    m = x.mean(dim=axis, keepdim=True)
    v = x.var(dim=axis, keepdim=True, unbiased=False)
    return (x - m) / torch.sqrt(v + eps)
def _scatter_add(tgt, idx, src, axis=0):
    return tgt.clone().index_add_(axis, idx, src)
def _segment_sum(src, seg, n):
    out = torch.zeros((n,) + src.shape[1:], dtype=src.dtype)
    return out.index_add_(0, seg, src)
def _pad(x, pw):
    flat = tuple(v for pair in reversed(pw) for v in pair)
    return torch.nn.functional.pad(x, flat)

kernel_name = 'concat_heads__gqa_block_cached__gqa_block_cached__decode'

def setup_inputs():

    _w = (lambda a: (lambda t: t.to(torch.bfloat16) if t.is_floating_point() else t)(torch.as_tensor(a)))
    def _A_setup():
        B, Sq, D = 1, 1, 640
        Hq, Hkv, Sk, Dh = 32, 32, 32768, 192
        assert Hq % Hkv == 0, (Hq, Hkv)
        return {
            "x": _w((lambda *s: torch.randn(*s, dtype=torch.bfloat16) * 0.8)(B, Sq, D)),
            "k_cache": _w((lambda *s: torch.randn(*s, dtype=torch.bfloat16) * 0.8)(B, Hkv, Sk, Dh)),
            "v_cache": _w((lambda *s: torch.randn(*s, dtype=torch.bfloat16) * 0.05)(B, Hkv, Sk, Dh)),
            "wq": _w((lambda *s: torch.randn(*s, dtype=torch.bfloat16) * 0.8)(D, Hq * Dh) * (D ** -0.5)),
            "wo": _w((lambda *s: torch.randn(*s, dtype=torch.bfloat16) * 0.05)(Hq * Dh, D) * ((Hq * Dh) ** -0.5)),
        }
    _A_in = _A_setup()

    def _B_setup():
        B, Sq, D = 1, 1, 640
        Hq, Hkv, Sk, Dh = 32, 32, 32768, 192
        assert Hq % Hkv == 0, (Hq, Hkv)
        return {
            "x": _w((lambda *s: torch.randn(*s, dtype=torch.bfloat16) * 0.8)(B, Sq, D)),
            "k_cache": _w((lambda *s: torch.randn(*s, dtype=torch.bfloat16) * 0.8)(B, Hkv, Sk, Dh)),
            "v_cache": _w((lambda *s: torch.randn(*s, dtype=torch.bfloat16) * 0.05)(B, Hkv, Sk, Dh)),
            "wq": _w((lambda *s: torch.randn(*s, dtype=torch.bfloat16) * 0.8)(D, Hq * Dh) * (D ** -0.5)),
            "wo": _w((lambda *s: torch.randn(*s, dtype=torch.bfloat16) * 0.05)(Hq * Dh, D) * ((Hq * Dh) ** -0.5)),
        }
    _B_in = _B_setup()

    return {
        "A_x": _A_in["x"],
        "A_k_cache": _A_in["k_cache"],
        "A_v_cache": _A_in["v_cache"],
        "A_wq": _A_in["wq"],
        "A_wo": _A_in["wo"],
        "B_x": _B_in["x"],
        "B_k_cache": _B_in["k_cache"],
        "B_v_cache": _B_in["v_cache"],
        "B_wq": _B_in["wq"],
        "B_wo": _B_in["wo"],
    }

def reference(A_x, A_k_cache, A_v_cache, A_wq, A_wo, B_x, B_k_cache, B_v_cache, B_wq, B_wo):
    def _A_ref(x, k_cache, v_cache, wq, wo):
        B, Sq, D = x.shape
        Hq, Dh = 32, 192
        Hkv = k_cache.shape[1]
        rep = Hq // Hkv
        q = (lambda x, *d: x.permute(*d))((x @ wq).reshape(B, Sq, Hq, Dh), 0, 2, 1, 3)
        k = (lambda x, n, axis: torch.repeat_interleave(x, n, dim=axis))(k_cache, rep, axis=1)
        v = (lambda x, n, axis: torch.repeat_interleave(x, n, dim=axis))(v_cache, rep, axis=1)
        scores = torch.einsum("bhqd,bhkd->bhqk", q, k) * (Dh ** -0.5)
        p = _softmax(scores, axis=-1)
        o = torch.einsum("bhqk,bhkd->bhqd", p, v)
        return (lambda x, *d: x.permute(*d))(o, 0, 2, 1, 3).reshape(B, Sq, Hq * Dh) @ wo

    def _B_ref(x, k_cache, v_cache, wq, wo):
        B, Sq, D = x.shape
        Hq, Dh = 32, 192
        Hkv = k_cache.shape[1]
        rep = Hq // Hkv
        q = (lambda x, *d: x.permute(*d))((x @ wq).reshape(B, Sq, Hq, Dh), 0, 2, 1, 3)
        k = (lambda x, n, axis: torch.repeat_interleave(x, n, dim=axis))(k_cache, rep, axis=1)
        v = (lambda x, n, axis: torch.repeat_interleave(x, n, dim=axis))(v_cache, rep, axis=1)
        scores = torch.einsum("bhqd,bhkd->bhqk", q, k) * (Dh ** -0.5)
        p = _softmax(scores, axis=-1)
        o = torch.einsum("bhqk,bhkd->bhqd", p, v)
        return (lambda x, *d: x.permute(*d))(o, 0, 2, 1, 3).reshape(B, Sq, Hq * Dh) @ wo

    a = _A_ref(A_x, A_k_cache, A_v_cache, A_wq, A_wo)
    b = _B_ref(B_x, B_k_cache, B_v_cache, B_wq, B_wo)
    return torch.concatenate([a, b], axis=-1)
```

```cpp
#include <hip/hip_runtime.h>
#include <hip/hip_bf16.h>
#include <stdint.h>

typedef unsigned u32x4 __attribute__((ext_vector_type(4)));
typedef float f32x4 __attribute__((ext_vector_type(4)));
typedef __bf16 bf16x2 __attribute__((ext_vector_type(2)));
#define GAS __attribute__((address_space(1)))

constexpr int D = 640, H = 32, SK = 32768, DH = 192, HD = H * DH;
#define NSPLIT 4
#define NWAVES 8
#define RING 4
#define KVAUX 2
constexpr int NTHR = NWAVES * 64;
constexpr int TOK_PER_WG = SK / NSPLIT;
constexpr int STEPS_PER_WAVE = TOK_PER_WG / 8 / NWAVES;
static_assert(STEPS_PER_WAVE % RING == 0, "ring");

struct Params {
  const uint16_t* x[2];
  const uint16_t* k[2];
  const uint16_t* v[2];
  const uint16_t* wq[2];
  const uint16_t* wo[2];
  uint16_t* out;
  float* ws;
};
constexpr int WS_Q = 0, WS_P = 2 * HD, PREC = 256;

__device__ __forceinline__ float bf2f(unsigned short b) { return __builtin_bit_cast(float, (unsigned)b << 16); }
__device__ __forceinline__ float lo2f(unsigned u) { return __builtin_bit_cast(float, u << 16); }
__device__ __forceinline__ float hi2f(unsigned u) { return __builtin_bit_cast(float, u & 0xffff0000u); }
__device__ __forceinline__ unsigned short f2bf(float f) {
  unsigned u = __builtin_bit_cast(unsigned, f);
  u += 0x7FFFu + ((u >> 16) & 1u);
  return (unsigned short)(u >> 16);
}
__device__ __forceinline__ float dot2(unsigned k, unsigned q, float acc) {
  return __builtin_amdgcn_fdot2_f32_bf16(__builtin_bit_cast(bf16x2, k), __builtin_bit_cast(bf16x2, q), acc, false);
}
template <int CTRL> __device__ __forceinline__ float dpp(float x) {
  return __builtin_bit_cast(float, __builtin_amdgcn_mov_dpp(__builtin_bit_cast(int, x), CTRL, 0xf, 0xf, true));
}
constexpr int XOR1 = 0xB1, XOR2 = 0x4E, XOR7 = 0x141;

__global__ void __launch_bounds__(256) k_qproj(Params p) {
  __shared__ float red[32][64];
  const int br = blockIdx.x / 96, cb = blockIdx.x % 96;
  const int tid = threadIdx.x, c8 = tid & 7, rr = tid >> 3;
  const uint16_t* wq = p.wq[br] + cb * 64 + c8 * 8;
  const uint16_t* x = p.x[br];
  u32x4 w[20];
#pragma unroll
  for (int i = 0; i < 20; ++i) w[i] = __builtin_nontemporal_load((const GAS u32x4*)(wq + (size_t)(rr + 32 * i) * HD));
  float acc[8] = {0, 0, 0, 0, 0, 0, 0, 0};
#pragma unroll
  for (int i = 0; i < 20; ++i) {
    const float xv = bf2f(x[rr + 32 * i]);
#pragma unroll
    for (int j = 0; j < 4; ++j) { acc[2 * j] = fmaf(xv, lo2f(w[i][j]), acc[2 * j]); acc[2 * j + 1] = fmaf(xv, hi2f(w[i][j]), acc[2 * j + 1]); }
  }
#pragma unroll
  for (int j = 0; j < 8; ++j) red[rr][c8 * 8 + j] = acc[j];
  __syncthreads();
  if (tid < 64) {
    float s = 0.f;
#pragma unroll
    for (int r = 0; r < 32; ++r) s += red[r][tid];
    p.ws[WS_Q + br * HD + cb * 64 + tid] = bf2f(f2bf(s));
  }
}

__global__ void __launch_bounds__(NTHR) k_attn(Params p) {
  __shared__ float sm_acc[NWAVES * 8][DH + 4];
  __shared__ float sm_ml[NWAVES * 8][2];
  const int bid = blockIdx.x;
  const int hh = bid / NSPLIT, sp = bid % NSPLIT;
  const int br = hh >> 5, h = hh & 31;
  const int tid = threadIdx.x, lane = tid & 63, wave = __builtin_amdgcn_readfirstlane(tid >> 6);
  const int c8 = lane & 7, tt = lane >> 3;
  unsigned qf[12];
  {
    const float* q = p.ws + WS_Q + br * HD + h * DH;
#pragma unroll
    for (int i = 0; i < 3; ++i)
#pragma unroll
      for (int j = 0; j < 4; ++j) {
        const float a = q[64 * i + 8 * c8 + 2 * j], b = q[64 * i + 8 * c8 + 2 * j + 1];
        qf[i * 4 + j] = (__builtin_bit_cast(unsigned, a) >> 16) | (__builtin_bit_cast(unsigned, b) & 0xffff0000u);
      }
  }
  const size_t base = ((size_t)h * SK + (size_t)sp * TOK_PER_WG) * DH;
  const __amdgpu_buffer_rsrc_t rk = __builtin_amdgcn_make_buffer_rsrc((void*)(p.k[br] + base), 0, TOK_PER_WG * DH * 2, 0x00020000);
  const __amdgpu_buffer_rsrc_t rv = __builtin_amdgcn_make_buffer_rsrc((void*)(p.v[br] + base), 0, TOK_PER_WG * DH * 2, 0x00020000);
  const int voff = tt * 384 + c8 * 16;
  constexpr int STEPB = 3072 * NWAVES;
  int soff = wave * 3072;
  u32x4 K[RING][3], V[RING][3];
#pragma unroll
  for (int r = 0; r < RING; ++r) {
#pragma unroll
    for (int i = 0; i < 3; ++i) K[r][i] = __builtin_amdgcn_raw_buffer_load_b128(rk, voff + 128 * i, soff + r * STEPB, KVAUX);
#pragma unroll
    for (int i = 0; i < 3; ++i) V[r][i] = __builtin_amdgcn_raw_buffer_load_b128(rv, voff + 128 * i, soff + r * STEPB, KVAUX);
  }
  __builtin_amdgcn_sched_barrier(0);
  constexpr float C2 = 0.07216878364870322f * 1.4426950408889634f;
  float m = -INFINITY, l = 0.f;
  float acc[24];
#pragma unroll
  for (int j = 0; j < 24; ++j) acc[j] = 0.f;
  for (int it = 0; it < STEPS_PER_WAVE / RING; ++it) {
    const int nsoff = soff + RING * STEPB;
#pragma unroll
    for (int r = 0; r < RING; ++r) {
      float s0 = 0.f, s1 = 0.f, s2 = 0.f;
#pragma unroll
      for (int j = 0; j < 4; ++j) { s0 = dot2(K[r][0][j], qf[j], s0); s1 = dot2(K[r][1][j], qf[4 + j], s1); s2 = dot2(K[r][2][j], qf[8 + j], s2); }
      float s = (s0 + s1) + s2;
      s += dpp<XOR1>(s); s += dpp<XOR2>(s); s += dpp<XOR7>(s);
      s *= C2;
#pragma unroll
      for (int i = 0; i < 3; ++i) K[r][i] = __builtin_amdgcn_raw_buffer_load_b128(rk, voff + 128 * i, nsoff + r * STEPB, KVAUX);
      if (__any(s > m)) {
        const float mn = fmaxf(m, s);
        const float al = __builtin_amdgcn_exp2f(m - mn);
        l *= al;
#pragma unroll
        for (int j = 0; j < 24; ++j) acc[j] *= al;
        m = mn;
      }
      const float pr = __builtin_amdgcn_exp2f(s - m);
      l += pr;
#pragma unroll
      for (int i = 0; i < 3; ++i)
#pragma unroll
        for (int j = 0; j < 4; ++j) {
          acc[i * 8 + 2 * j] = fmaf(pr, lo2f(V[r][i][j]), acc[i * 8 + 2 * j]);
          acc[i * 8 + 2 * j + 1] = fmaf(pr, hi2f(V[r][i][j]), acc[i * 8 + 2 * j + 1]);
        }
#pragma unroll
      for (int i = 0; i < 3; ++i) V[r][i] = __builtin_amdgcn_raw_buffer_load_b128(rv, voff + 128 * i, nsoff + r * STEPB, KVAUX);
      __builtin_amdgcn_sched_barrier(0);
    }
    soff = nsoff;
  }
  const int g = wave * 8 + tt;
#pragma unroll
  for (int i = 0; i < 3; ++i)
#pragma unroll
    for (int j = 0; j < 8; ++j) sm_acc[g][64 * i + 8 * c8 + j] = acc[i * 8 + j];
  if (c8 == 0) { sm_ml[g][0] = m; sm_ml[g][1] = l; }
  __syncthreads();
  if (tid < DH) {
    float M = -INFINITY;
    for (int q = 0; q < NWAVES * 8; ++q) M = fmaxf(M, sm_ml[q][0]);
    float L = 0.f, o = 0.f;
    for (int q = 0; q < NWAVES * 8; ++q) { const float w = __builtin_amdgcn_exp2f(sm_ml[q][0] - M); L = fmaf(w, sm_ml[q][1], L); o = fmaf(w, sm_acc[q][tid], o); }
    float* rec = p.ws + WS_P + (size_t)(hh * NSPLIT + sp) * PREC;
    rec[64 + tid] = o;
    if (tid == 0) { rec[0] = M; rec[1] = L; }
  }
}

__global__ void __launch_bounds__(256) k_out(Params p) {
  __shared__ float o_s[HD];
  __shared__ float red[256][9];
  const int br = blockIdx.x / 80, cb = blockIdx.x % 80;
  const int tid = threadIdx.x;
  const uint16_t* wo = p.wo[br] + cb * 8;
  u32x4 w[24];
#pragma unroll
  for (int i = 0; i < 24; ++i) w[i] = *(const GAS u32x4*)(wo + (size_t)(tid + 256 * i) * D);
  for (int e = tid; e < HD; e += 256) {
    const int h = e / DH, d = e % DH;
    const float* rec = p.ws + WS_P + (size_t)((br * 32 + h) * NSPLIT) * PREC;
    float M = -INFINITY;
#pragma unroll
    for (int s = 0; s < NSPLIT; ++s) M = fmaxf(M, rec[s * PREC]);
    float L = 0.f, o = 0.f;
#pragma unroll
    for (int s = 0; s < NSPLIT; ++s) { const float ww = __builtin_amdgcn_exp2f(rec[s * PREC] - M); L = fmaf(ww, rec[s * PREC + 1], L); o = fmaf(ww, rec[s * PREC + 64 + d], o); }
    o_s[e] = bf2f(f2bf(o / L));
  }
  __syncthreads();
  float acc[8] = {0, 0, 0, 0, 0, 0, 0, 0};
#pragma unroll
  for (int i = 0; i < 24; ++i) {
    const float ov = o_s[tid + 256 * i];
#pragma unroll
    for (int j = 0; j < 4; ++j) { acc[2 * j] = fmaf(ov, lo2f(w[i][j]), acc[2 * j]); acc[2 * j + 1] = fmaf(ov, hi2f(w[i][j]), acc[2 * j + 1]); }
  }
#pragma unroll
  for (int j = 0; j < 8; ++j) red[tid][j] = acc[j];
  __syncthreads();
  if (tid < 64) {
    const int j = tid & 7, part = tid >> 3;
    float s = 0.f;
    for (int r = 0; r < 32; ++r) s += red[part * 32 + r][j];
    s += __shfl_xor(s, 8); s += __shfl_xor(s, 16); s += __shfl_xor(s, 32);
    if (tid < 8) p.out[br * D + cb * 8 + j] = f2bf(s);
  }
}

extern "C" void kernel_launch(void* const* d_in, const int* in_sizes, int n_in, void* d_out, int out_size, void* d_ws, size_t ws_size, hipStream_t stream) {
  Params p;
  for (int b = 0; b < 2; ++b) {
    p.x[b] = (const uint16_t*)d_in[5 * b + 0];
    p.k[b] = (const uint16_t*)d_in[5 * b + 1];
    p.v[b] = (const uint16_t*)d_in[5 * b + 2];
    p.wq[b] = (const uint16_t*)d_in[5 * b + 3];
    p.wo[b] = (const uint16_t*)d_in[5 * b + 4];
  }
  p.out = (uint16_t*)d_out;
  p.ws = (float*)d_ws;
  k_qproj<<<192, 256, 0, stream>>>(p);
  k_attn<<<64 * NSPLIT, NTHR, 0, stream>>>(p);
  k_out<<<160, 256, 0, stream>>>(p);
}
```

```cpp
#include <hip/hip_runtime.h>
#include <hip/hip_bf16.h>
#include <stdint.h>

typedef unsigned u32x4 __attribute__((ext_vector_type(4)));
typedef unsigned u32x2 __attribute__((ext_vector_type(2)));
typedef unsigned u32x4a8 __attribute__((ext_vector_type(4), aligned(8)));
typedef __bf16 bf16x2 __attribute__((ext_vector_type(2)));
#define GAS __attribute__((address_space(1)))

constexpr int D = 640, SK = 32768, DH = 192, HD = 32 * DH;
constexpr int NWG = 256, NWAVES = 4, NTHR = 256;
constexpr int NG = 8, WPG = 32, HPG = 8;
constexpr int WV = WPG * NWAVES;
constexpr int ITH = SK / 8 / WV;
constexpr int STEPB = 3072 * WV;
#define NX 3
#define KVAUX 2
constexpr int AUX_SC1 = 16;
constexpr unsigned SPIN_CAP = 1u << 20;

constexpr int WS_EPOCH = 0;
constexpr int WS_Q = 4096;
constexpr int WS_H1 = 65536;
constexpr int WS_H2 = WS_H1 + NWG * 16384;
constexpr int WS_END = WS_H2 + NWG * 5120;

constexpr int PREC = 196;
constexpr int L_REC = 0;
constexpr int L_Q = L_REC + HPG * NWAVES * PREC * 4;
constexpr int L_RED = L_Q + HPG * 96 * 4;
constexpr int L_END = L_RED + NWAVES * D * 4;
constexpr int LDS_BYTES = 84 * 1024;
static_assert(L_END <= LDS_BYTES, "lds");

struct Params {
  const uint16_t* x[2];
  const uint16_t* k[2];
  const uint16_t* v[2];
  const uint16_t* wq[2];
  const uint16_t* wo[2];
  uint16_t* out;
  unsigned char* ws;
};

__device__ __forceinline__ float lo2f(unsigned u) { return __uint_as_float(u << 16); }
__device__ __forceinline__ float hi2f(unsigned u) { return __uint_as_float(u & 0xffff0000u); }
__device__ __forceinline__ unsigned f2bf(float f) {
  unsigned u = __float_as_uint(f);
  u += 0x7FFFu + ((u >> 16) & 1u);
  return u >> 16;
}
__device__ __forceinline__ float dot2(unsigned k, unsigned q, float acc) {
  return __builtin_amdgcn_fdot2_f32_bf16(__builtin_bit_cast(bf16x2, k), __builtin_bit_cast(bf16x2, q), acc, false);
}
template <int CTRL> __device__ __forceinline__ float dpp(float x) {
  return __int_as_float(__builtin_amdgcn_mov_dpp(__float_as_int(x), CTRL, 0xf, 0xf, true));
}
constexpr int XOR1 = 0xB1, XOR2 = 0x4E, XOR7 = 0x141, ROR8 = 0x128;
__device__ __forceinline__ float swap32_sum(float a, float b) {
  auto s = __builtin_amdgcn_permlane32_swap(__float_as_uint(a), __float_as_uint(b), false, false);
  return __uint_as_float(s[0]) + __uint_as_float(s[1]);
}
__device__ __forceinline__ float swap16_sum(float a, float b) {
  auto s = __builtin_amdgcn_permlane16_swap(__float_as_uint(a), __float_as_uint(b), false, false);
  return __uint_as_float(s[0]) + __uint_as_float(s[1]);
}
__device__ __forceinline__ float swap32_max(float a) {
  auto s = __builtin_amdgcn_permlane32_swap(__float_as_uint(a), __float_as_uint(a), false, false);
  return fmaxf(__uint_as_float(s[0]), __uint_as_float(s[1]));
}
__device__ __forceinline__ float swap16_max(float a) {
  auto s = __builtin_amdgcn_permlane16_swap(__float_as_uint(a), __float_as_uint(a), false, false);
  return fmaxf(__uint_as_float(s[0]), __uint_as_float(s[1]));
}
__device__ __forceinline__ float sum32(float x) {
  x += dpp<XOR1>(x); x += dpp<XOR2>(x); x += dpp<XOR7>(x); x += dpp<ROR8>(x);
  return swap16_sum(x, x);
}
__device__ __forceinline__ float max32(float x) {
  x = fmaxf(x, dpp<XOR1>(x)); x = fmaxf(x, dpp<XOR2>(x)); x = fmaxf(x, dpp<XOR7>(x)); x = fmaxf(x, dpp<ROR8>(x));
  return swap16_max(x);
}
__device__ __forceinline__ bool tags_ok(const u32x4& r, unsigned ta, unsigned tb) { return r[1] == ta && r[3] == tb; }
__device__ __forceinline__ u32x4 mkrec(unsigned a, unsigned b, unsigned ta, unsigned tb) { return u32x4{a, ta, b, tb}; }

struct KV { u32x4 k[3], v[3]; };
struct State { float m, l, acc[24]; };

__global__ void __launch_bounds__(NTHR, 1) k_fused(Params p) {
  extern __shared__ __attribute__((aligned(16))) unsigned char smem[];
  float* const sm = (float*)(smem + L_REC);
  unsigned* const sq = (unsigned*)(smem + L_Q);
  float* const sred = (float*)(smem + L_RED);

  const int bid = blockIdx.x, g = bid / WPG, j = bid % WPG;
  const int br = g >> 2, h0 = (g & 3) * HPG;
  const int tid = threadIdx.x, lane = tid & 63, wave = __builtin_amdgcn_readfirstlane(tid >> 6);
  const int c8 = lane & 7, tt = lane >> 3;

  const __amdgpu_buffer_rsrc_t rws = __builtin_amdgcn_make_buffer_rsrc((void*)p.ws, 0, WS_END, 0x00020000);
  int eoff;
  asm volatile("v_mov_b32 %0, %1" : "=v"(eoff) : "s"(WS_EPOCH + br * 1024));
  const u32x2 ep = __builtin_amdgcn_raw_buffer_load_b64(rws, eoff, 0, AUX_SC1);

  u32x4 w[20];
  u32x4 xa, xb; u32x2 xc;
  const bool qprod = j < 24;
  if (qprod) {
    const int rr = tid >> 3;
    const uint16_t* x = p.x[br] + rr * 20;
    xa = *(const GAS u32x4a8*)(x);
    xb = *(const GAS u32x4a8*)(x + 8);
    xc = *(const GAS u32x2*)(x + 16);
    __builtin_amdgcn_sched_barrier(0);
    const uint16_t* wq = p.wq[br] + (size_t)(rr * 20) * HD + h0 * DH + j * 64 + c8 * 8;
#pragma unroll
    for (int i = 0; i < 20; ++i) w[i] = __builtin_nontemporal_load((const GAS u32x4*)(wq + (size_t)i * HD));
  }
  __builtin_amdgcn_sched_barrier(0);
  const size_t kvbase = (size_t)h0 * SK * DH;
  const __amdgpu_buffer_rsrc_t rk = __builtin_amdgcn_make_buffer_rsrc((void*)(p.k[br] + kvbase), 0, HPG * SK * DH * 2, 0x00020000);
  const __amdgpu_buffer_rsrc_t rv = __builtin_amdgcn_make_buffer_rsrc((void*)(p.v[br] + kvbase), 0, HPG * SK * DH * 2, 0x00020000);
  const int v0 = tt * 384 + c8 * 16, v1 = v0 + 128, v2 = v0 + 256;
  unsigned soff = (j * NWAVES + wave) * 3072;
#define LDK(S, so) { S.k[0] = __builtin_amdgcn_raw_buffer_load_b128(rk, v0, (so), KVAUX); S.k[1] = __builtin_amdgcn_raw_buffer_load_b128(rk, v1, (so), KVAUX); S.k[2] = __builtin_amdgcn_raw_buffer_load_b128(rk, v2, (so), KVAUX); }
#define LDV(S, so) { S.v[0] = __builtin_amdgcn_raw_buffer_load_b128(rv, v0, (so), KVAUX); S.v[1] = __builtin_amdgcn_raw_buffer_load_b128(rv, v1, (so), KVAUX); S.v[2] = __builtin_amdgcn_raw_buffer_load_b128(rv, v2, (so), KVAUX); }
  KV X[NX > 0 ? NX : 1], S;
#pragma unroll
  for (int n = 0; n < NX; ++n) { LDK(X[n], soff + n * STEPB); LDV(X[n], soff + n * STEPB); __builtin_amdgcn_sched_barrier(0); }
  soff += NX * STEPB;
  LDK(S, soff); LDV(S, soff);
  __builtin_amdgcn_sched_barrier(0);

  const unsigned long long epoch = ((unsigned long long)ep[1] << 32) | ep[0];
  unsigned long long z = epoch + 0x9E3779B97F4A7C15ull + 1ull;
  z = (z ^ (z >> 30)) * 0xBF58476D1CE4E5B9ull; z = (z ^ (z >> 27)) * 0x94D049BB133111EBull; z ^= z >> 31;
  const unsigned ta = (unsigned)z, tb = (unsigned)(z >> 32);

  if (qprod) {
    float a[8] = {0, 0, 0, 0, 0, 0, 0, 0};
    const unsigned xs[10] = {xa[0], xa[1], xa[2], xa[3], xb[0], xb[1], xb[2], xb[3], xc[0], xc[1]};
#pragma unroll
    for (int i = 0; i < 20; ++i) {
      const float xv = (i & 1) ? hi2f(xs[i >> 1]) : lo2f(xs[i >> 1]);
#pragma unroll
      for (int e = 0; e < 4; ++e) { a[2 * e] = fmaf(xv, lo2f(w[i][e]), a[2 * e]); a[2 * e + 1] = fmaf(xv, hi2f(w[i][e]), a[2 * e + 1]); }
    }
    float r4[4], r2[2];
#pragma unroll
    for (int e = 0; e < 4; ++e) r4[e] = swap32_sum(a[e], a[e + 4]);
#pragma unroll
    for (int e = 0; e < 2; ++e) r2[e] = swap16_sum(r4[e], r4[e + 2]);
    const float u0 = r2[0] + dpp<ROR8>(r2[0]), u1 = r2[1] + dpp<ROR8>(r2[1]);
    const int e = (lane >= 32 ? 4 : 0) + ((lane & 16) ? 2 : 0) + ((lane & 8) ? 1 : 0);
    sred[wave * 64 + c8 * 8 + e] = (lane & 8) ? u1 : u0;
  }
  __syncthreads();
  if (qprod && wave == 0) {
    const float s = (sred[lane] + sred[64 + lane]) + (sred[128 + lane] + sred[192 + lane]);
    const float b = __uint_as_float(f2bf(s));
    const unsigned b0 = __float_as_uint(dpp<0x00>(b)), b1 = __float_as_uint(dpp<0x55>(b)), b2 = __float_as_uint(dpp<0xAA>(b)), b3 = __float_as_uint(dpp<0xFF>(b));
    if ((lane & 3) == 0)
      __builtin_amdgcn_raw_buffer_store_b128(mkrec(b0 | (b1 << 16), b2 | (b3 << 16), ta, tb), rws, WS_Q + (g * 384 + j * 16 + (lane >> 2)) * 16, 0, AUX_SC1);
  }
  {
    const int r0 = wave * 96 + lane, r1 = wave * 96 + 64 + (lane & 31);
    u32x4 a, b;
    for (unsigned spin = 0; spin < SPIN_CAP; ++spin) {
      asm volatile("" ::: "memory");
      a = __builtin_amdgcn_raw_buffer_load_b128(rws, WS_Q + (g * 384 + r0) * 16, 0, AUX_SC1);
      b = __builtin_amdgcn_raw_buffer_load_b128(rws, WS_Q + (g * 384 + r1) * 16, 0, AUX_SC1);
      if (__all(tags_ok(a, ta, tb) && tags_ok(b, ta, tb))) break;
      __builtin_amdgcn_s_sleep(2);
    }
    *(u32x2*)&sq[2 * r0] = u32x2{a[0], a[2]};
    *(u32x2*)&sq[2 * r1] = u32x2{b[0], b[2]};
  }
  __syncthreads();

  constexpr float C2 = 0.07216878364870322f * 1.4426950408889634f;
  State st;
  u32x4 qf[3];
  auto load_q = [&](int hd) {
#pragma unroll
    for (int i = 0; i < 3; ++i) qf[i] = *(const u32x4*)&sq[hd * 96 + 32 * i + 4 * c8];
    st.m = -INFINITY; st.l = 0.f;
#pragma unroll
    for (int a = 0; a < 24; ++a) st.acc[a] = 0.f;
  };
  auto scores = [&](const KV& T) {
    float s0 = 0.f, s1 = 0.f, s2 = 0.f;
#pragma unroll
    for (int e = 0; e < 4; ++e) { s0 = dot2(T.k[0][e], qf[0][e], s0); s1 = dot2(T.k[1][e], qf[1][e], s1); s2 = dot2(T.k[2][e], qf[2][e], s2); }
    return (s0 + s1) + s2;
  };
  auto softmax_pv = [&](float s, const KV& T) {
    s += dpp<XOR1>(s); s += dpp<XOR2>(s); s += dpp<XOR7>(s);
    s *= C2;
    if (__any(s > st.m)) {
      float mn = fmaxf(st.m, s);
      mn = fmaxf(mn, dpp<ROR8>(mn)); mn = swap16_max(mn); mn = swap32_max(mn);
      const float al = __builtin_amdgcn_exp2f(st.m - mn);
      st.l *= al;
#pragma unroll
      for (int a = 0; a < 24; ++a) st.acc[a] *= al;
      st.m = mn;
    }
    const float pr = __builtin_amdgcn_exp2f(s - st.m);
    st.l += pr;
#pragma unroll
    for (int i = 0; i < 3; ++i)
#pragma unroll
      for (int e = 0; e < 4; ++e) {
        st.acc[i * 8 + 2 * e] = fmaf(pr, lo2f(T.v[i][e]), st.acc[i * 8 + 2 * e]);
        st.acc[i * 8 + 2 * e + 1] = fmaf(pr, hi2f(T.v[i][e]), st.acc[i * 8 + 2 * e + 1]);
      }
  };
  auto step = [&]() {
    const unsigned ns = soff + STEPB;
    const float s = scores(S);
    __builtin_amdgcn_sched_barrier(0);
    LDK(S, ns);
    __builtin_amdgcn_sched_barrier(0);
    softmax_pv(s, S);
    __builtin_amdgcn_sched_barrier(0);
    LDV(S, ns);
    __builtin_amdgcn_sched_barrier(0);
    soff = ns;
  };
  auto flush = [&](int hd) {
    float r12[12], r6[6], r3[3];
#pragma unroll
    for (int a = 0; a < 12; ++a) r12[a] = swap32_sum(st.acc[a], st.acc[a + 12]);
#pragma unroll
    for (int a = 0; a < 6; ++a) r6[a] = swap16_sum(r12[a], r12[a + 6]);
#pragma unroll
    for (int a = 0; a < 3; ++a) {
      const float u = r6[a] + dpp<ROR8>(r6[a]), v = r6[a + 3] + dpp<ROR8>(r6[a + 3]);
      r3[a] = (lane & 8) ? v : u;
    }
    float l = st.l + dpp<ROR8>(st.l);
    l = swap16_sum(l, l); l = swap32_sum(l, l);
    float* rec = sm + (hd * NWAVES + wave) * PREC;
    const int abase = (lane >= 32 ? 12 : 0) + ((lane & 16) ? 6 : 0) + ((lane & 8) ? 3 : 0);
#pragma unroll
    for (int a = 0; a < 3; ++a) { const int ai = abase + a; rec[4 + 64 * (ai >> 3) + 8 * c8 + (ai & 7)] = r3[a]; }
    if (lane == 0) { rec[0] = st.m; rec[1] = l; }
  };

  load_q(0);
#pragma unroll
  for (int n = 0; n < NX; ++n) { const float s = scores(X[n]); softmax_pv(s, X[n]); __builtin_amdgcn_sched_barrier(0); }
  for (int hd = 0; hd < HPG - 1; ++hd) {
    const int n = hd == 0 ? ITH - NX : ITH;
    for (int it = 0; it < n; ++it) step();
    flush(hd);
    load_q(hd + 1);
  }
  for (int it = 0; it < ITH - 1; ++it) step();
  const int thd = j >> 2, trow = (h0 + thd) * DH + (j & 3) * 48 + wave * 12;
  u32x4 wa[12]; unsigned wb[12];
  {
    const float s = scores(S);
    __builtin_amdgcn_sched_barrier(0);
    const __amdgpu_buffer_rsrc_t rwo = __builtin_amdgcn_make_buffer_rsrc((void*)(p.wo[br] + (size_t)trow * D), 0, 12 * D * 2, 0x00020000);
#pragma unroll
    for (int r = 0; r < 12; ++r) {
      wa[r] = __builtin_amdgcn_raw_buffer_load_b128(rwo, lane * 20, r * D * 2, KVAUX);
      wb[r] = __builtin_amdgcn_raw_buffer_load_b32(rwo, lane * 20 + 16, r * D * 2, KVAUX);
    }
    __builtin_amdgcn_sched_barrier(0);
    softmax_pv(s, S);
  }
  flush(HPG - 1);
  __syncthreads();

#pragma unroll
  for (int i = 0; i < 4; ++i) {
    const int R = tid + 256 * i, hd = R >> 7, sl = (R >> 3) & 15, k = R & 7;
    const float* rec = sm + hd * NWAVES * PREC;
    const float m0 = rec[0], m1 = rec[PREC], m2 = rec[2 * PREC], m3 = rec[3 * PREC];
    const float M = fmaxf(fmaxf(m0, m1), fmaxf(m2, m3));
    const float e0 = __builtin_amdgcn_exp2f(m0 - M), e1 = __builtin_amdgcn_exp2f(m1 - M), e2 = __builtin_amdgcn_exp2f(m2 - M), e3 = __builtin_amdgcn_exp2f(m3 - M);
    const bool hdr = (k == 0 || k == 7);
    const int ia = hdr ? 1 : 4 + 12 * sl + 2 * (k - 1), ib = hdr ? 1 : ia + 1;
    const float va = (e0 * rec[ia] + e1 * rec[PREC + ia]) + (e2 * rec[2 * PREC + ia] + e3 * rec[3 * PREC + ia]);
    const float vb = (e0 * rec[ib] + e1 * rec[PREC + ib]) + (e2 * rec[2 * PREC + ib] + e3 * rec[3 * PREC + ib]);
    __builtin_amdgcn_raw_buffer_store_b128(mkrec(__float_as_uint(hdr ? M : va), __float_as_uint(vb), ta, tb), rws, WS_H1 + bid * 16384 + R * 16, 0, AUX_SC1);
  }
  float o6[3][2];
  {
    const int jj = lane & 31, half = lane >> 5;
    const int off = WS_H1 + (g * WPG + jj) * 16384 + thd * 2048 + ((j & 3) * 4 + wave) * 128 + half * 64;
    u32x4 r[4];
    for (unsigned spin = 0; spin < SPIN_CAP; ++spin) {
      asm volatile("" ::: "memory");
#pragma unroll
      for (int i = 0; i < 4; ++i) r[i] = __builtin_amdgcn_raw_buffer_load_b128(rws, off + 16 * i, 0, AUX_SC1);
      if (__all(tags_ok(r[0], ta, tb) && tags_ok(r[1], ta, tb) && tags_ok(r[2], ta, tb) && tags_ok(r[3], ta, tb))) break;
      __builtin_amdgcn_s_sleep(2);
    }
    const float mj = __uint_as_float(half ? r[3][0] : r[0][0]), lj = __uint_as_float(half ? r[3][2] : r[0][2]);
    const float M = max32(mj);
    const float wj = __builtin_amdgcn_exp2f(mj - M);
    const float inv = 1.f / sum32(wj * lj);
#pragma unroll
    for (int i = 0; i < 3; ++i) {
      const u32x4 q = half ? r[i] : r[i + 1];
      o6[i][0] = __uint_as_float(f2bf(sum32(wj * __uint_as_float(q[0])) * inv) << 16);
      o6[i][1] = __uint_as_float(f2bf(sum32(wj * __uint_as_float(q[2])) * inv) << 16);
    }
  }
  {
    float y[10] = {0, 0, 0, 0, 0, 0, 0, 0, 0, 0};
#pragma unroll
    for (int r = 0; r < 12; ++r) {
      const float ov = __int_as_float(__builtin_amdgcn_readlane(__float_as_int(o6[(r % 6) >> 1][r & 1]), r < 6 ? 0 : 32));
#pragma unroll
      for (int e = 0; e < 4; ++e) { y[2 * e] = fmaf(ov, lo2f(wa[r][e]), y[2 * e]); y[2 * e + 1] = fmaf(ov, hi2f(wa[r][e]), y[2 * e + 1]); }
      y[8] = fmaf(ov, lo2f(wb[r]), y[8]); y[9] = fmaf(ov, hi2f(wb[r]), y[9]);
    }
#pragma unroll
    for (int e = 0; e < 5; ++e) *(float2*)&sred[wave * D + lane * 10 + 2 * e] = make_float2(y[2 * e], y[2 * e + 1]);
  }
  __syncthreads();
#pragma unroll
  for (int i = 0; i < 2; ++i) {
    const int n = tid + 256 * i;
    if (n < 320) {
      const float a = (sred[2 * n] + sred[D + 2 * n]) + (sred[2 * D + 2 * n] + sred[3 * D + 2 * n]);
      const float b = (sred[2 * n + 1] + sred[D + 2 * n + 1]) + (sred[2 * D + 2 * n + 1] + sred[3 * D + 2 * n + 1]);
      __builtin_amdgcn_raw_buffer_store_b128(mkrec(__float_as_uint(a), __float_as_uint(b), ta, tb), rws, WS_H2 + bid * 5120 + n * 16, 0, AUX_SC1);
    }
  }
  const int b = (g & 3) * WPG + j;
  if (b < 40) {
    const int pp = wave * 32 + (lane & 31), half = lane >> 5;
    const int off = WS_H2 + (br * 128 + pp) * 5120 + (8 * b + 4 * half) * 16;
    u32x4 r[4];
    for (unsigned spin = 0; spin < SPIN_CAP; ++spin) {
      asm volatile("" ::: "memory");
#pragma unroll
      for (int i = 0; i < 4; ++i) r[i] = __builtin_amdgcn_raw_buffer_load_b128(rws, off + 16 * i, 0, AUX_SC1);
      if (__all(tags_ok(r[0], ta, tb) && tags_ok(r[1], ta, tb) && tags_ok(r[2], ta, tb) && tags_ok(r[3], ta, tb))) break;
      __builtin_amdgcn_s_sleep(2);
    }
    float* fin = sm;
#pragma unroll
    for (int i = 0; i < 4; ++i) {
      const float s0 = sum32(__uint_as_float(r[i][0])), s1 = sum32(__uint_as_float(r[i][2]));
      if ((lane & 31) == 0) { fin[wave * 16 + half * 8 + 2 * i] = s0; fin[wave * 16 + half * 8 + 2 * i + 1] = s1; }
    }
    __syncthreads();
    if (tid < 8) {
      const float a = (fin[2 * tid] + fin[16 + 2 * tid]) + (fin[32 + 2 * tid] + fin[48 + 2 * tid]);
      const float c = (fin[2 * tid + 1] + fin[16 + 2 * tid + 1]) + (fin[32 + 2 * tid + 1] + fin[48 + 2 * tid + 1]);
      ((unsigned*)p.out)[(br * D + 16 * b) / 2 + tid] = f2bf(a) | (f2bf(c) << 16);
    }
    if (b == 0 && tid == 0) __hip_atomic_store((GAS unsigned long long*)(p.ws + WS_EPOCH + br * 1024), epoch + 1ull, __ATOMIC_RELAXED, __HIP_MEMORY_SCOPE_AGENT);
  }
}

extern "C" void kernel_launch(void* const* d_in, const int* in_sizes, int n_in, void* d_out, int out_size, void* d_ws, size_t ws_size, hipStream_t stream) {
  Params p;
  for (int b = 0; b < 2; ++b) {
    p.x[b] = (const uint16_t*)d_in[5 * b + 0];
    p.k[b] = (const uint16_t*)d_in[5 * b + 1];
    p.v[b] = (const uint16_t*)d_in[5 * b + 2];
    p.wq[b] = (const uint16_t*)d_in[5 * b + 3];
    p.wo[b] = (const uint16_t*)d_in[5 * b + 4];
  }
  p.out = (uint16_t*)d_out;
  p.ws = (unsigned char*)d_ws;
  (void)hipFuncSetAttribute((const void*)k_fused, hipFuncAttributeMaxDynamicSharedMemorySize, LDS_BYTES);
  k_fused<<<NWG, NTHR, LDS_BYTES, stream>>>(p);
}
```

```cpp
#include <hip/hip_runtime.h>
#include <hip/hip_bf16.h>
#include <stdint.h>

typedef unsigned u32x4 __attribute__((ext_vector_type(4)));
typedef unsigned u32x2 __attribute__((ext_vector_type(2)));
typedef unsigned u32x4a8 __attribute__((ext_vector_type(4), aligned(8)));
typedef float f32x4 __attribute__((ext_vector_type(4)));
typedef __bf16 bf16x2 __attribute__((ext_vector_type(2)));
#define GAS __attribute__((address_space(1)))

constexpr int D = 640, SK = 32768, DH = 192, HD = 32 * DH;
constexpr int NWG = 256, NWAVES = 4, NTHR = 256;
constexpr int WPG = 32;
constexpr unsigned HB = SK * DH * 2;
constexpr unsigned BUFB = 32u * HB;
constexpr unsigned ITB = 128u * 6144u;
#define KVAUX 2
#define KMODE 1
#define VMODE 1
#define KSLEEP 0
#define VSLEEP 0
constexpr int AUX_SC1 = 16;
constexpr unsigned SPIN_CAP = 1u << 20;

constexpr int WS_EPOCH = 0;
constexpr int WS_Q = 4096;
constexpr int WS_H1 = 65536;
constexpr int WS_H2 = WS_H1 + NWG * 16384;
constexpr int WS_END = WS_H2 + NWG * 5120;

constexpr int PREC = 196;
constexpr int L_REC = 0;
constexpr int L_Q = L_REC + 8 * NWAVES * PREC * 4;
constexpr int L_S = L_Q + 8 * 96 * 4;
constexpr int L_RED = L_S + NWAVES * 1024 * 4;
constexpr int L_END = L_RED + NWAVES * D * 4;
constexpr int LDS_BYTES = 84 * 1024;
static_assert(L_END <= LDS_BYTES, "lds");

struct Params {
  const uint16_t* x[2];
  const uint16_t* k[2];
  const uint16_t* v[2];
  const uint16_t* wq[2];
  const uint16_t* wo[2];
  uint16_t* out;
  unsigned char* ws;
};

__device__ __forceinline__ float lo2f(unsigned u) { return __uint_as_float(u << 16); }
__device__ __forceinline__ float hi2f(unsigned u) { return __uint_as_float(u & 0xffff0000u); }
__device__ __forceinline__ unsigned f2bf(float f) {
  unsigned u = __float_as_uint(f);
  u += 0x7FFFu + ((u >> 16) & 1u);
  return u >> 16;
}
__device__ __forceinline__ float dot2(unsigned k, unsigned q, float acc) {
  return __builtin_amdgcn_fdot2_f32_bf16(__builtin_bit_cast(bf16x2, k), __builtin_bit_cast(bf16x2, q), acc, false);
}
template <int CTRL> __device__ __forceinline__ float dpp(float x) {
  return __int_as_float(__builtin_amdgcn_mov_dpp(__float_as_int(x), CTRL, 0xf, 0xf, true));
}
constexpr int XOR1 = 0xB1, XOR2 = 0x4E, XOR7 = 0x141, ROR8 = 0x128;
__device__ __forceinline__ float swap32_sum(float a, float b) {
  auto s = __builtin_amdgcn_permlane32_swap(__float_as_uint(a), __float_as_uint(b), false, false);
  return __uint_as_float(s[0]) + __uint_as_float(s[1]);
}
__device__ __forceinline__ float swap16_sum(float a, float b) {
  auto s = __builtin_amdgcn_permlane16_swap(__float_as_uint(a), __float_as_uint(b), false, false);
  return __uint_as_float(s[0]) + __uint_as_float(s[1]);
}
__device__ __forceinline__ float swap32_max(float a) {
  auto s = __builtin_amdgcn_permlane32_swap(__float_as_uint(a), __float_as_uint(a), false, false);
  return fmaxf(__uint_as_float(s[0]), __uint_as_float(s[1]));
}
__device__ __forceinline__ float swap16_max(float a) {
  auto s = __builtin_amdgcn_permlane16_swap(__float_as_uint(a), __float_as_uint(a), false, false);
  return fmaxf(__uint_as_float(s[0]), __uint_as_float(s[1]));
}
__device__ __forceinline__ float sum32(float x) {
  x += dpp<XOR1>(x); x += dpp<XOR2>(x); x += dpp<XOR7>(x); x += dpp<ROR8>(x);
  return swap16_sum(x, x);
}
__device__ __forceinline__ float max32(float x) {
  x = fmaxf(x, dpp<XOR1>(x)); x = fmaxf(x, dpp<XOR2>(x)); x = fmaxf(x, dpp<XOR7>(x)); x = fmaxf(x, dpp<ROR8>(x));
  return swap16_max(x);
}
__device__ __forceinline__ bool tags_ok(const u32x4& r, unsigned ta, unsigned tb) { return r[1] == ta && r[3] == tb; }
__device__ __forceinline__ u32x4 mkrec(unsigned a, unsigned b, unsigned ta, unsigned tb) { return u32x4{a, ta, b, tb}; }

struct Slot { u32x4 d[3]; };

__global__ void __launch_bounds__(NTHR, 1) k_fused(Params p) {
  extern __shared__ __attribute__((aligned(16))) unsigned char smem[];
  float* const sm = (float*)(smem + L_REC);
  unsigned* const sq = (unsigned*)(smem + L_Q);
  float* const sred = (float*)(smem + L_RED);

  const int bid = blockIdx.x, g = bid / WPG, j = bid % WPG;
  const int tid = threadIdx.x, lane = tid & 63, wave = __builtin_amdgcn_readfirstlane(tid >> 6);
  const int c8 = lane & 7, tt = lane >> 3;
#define STAMP(i)
  STAMP(0);

  const __amdgpu_buffer_rsrc_t rws = __builtin_amdgcn_make_buffer_rsrc((void*)p.ws, 0, WS_END, 0x00020000);
  int eoff;
  asm volatile("v_mov_b32 %0, %1" : "=v"(eoff) : "s"(WS_EPOCH));
  const u32x2 ep = __builtin_amdgcn_raw_buffer_load_b64(rws, eoff, 0, AUX_SC1);

  u32x4 w[20];
  u32x4 xa, xb; u32x2 xc;
  const bool qprod = j < 24;
  if (qprod) {
    const int hs = j / 3, part = j % 3, qb = hs >> 2, qh = 8 * (hs & 3) + g;
    const int rr = tid >> 3;
    const uint16_t* x = p.x[qb] + rr * 20;
    xa = *(const GAS u32x4a8*)(x);
    xb = *(const GAS u32x4a8*)(x + 8);
    xc = *(const GAS u32x2*)(x + 16);
    __builtin_amdgcn_sched_barrier(0);
    const uint16_t* wq = p.wq[qb] + (size_t)(rr * 20) * HD + qh * DH + part * 64 + c8 * 8;
#pragma unroll
    for (int i = 0; i < 20; ++i) w[i] = __builtin_nontemporal_load((const GAS u32x4*)(wq + (size_t)i * HD));
  }
  __builtin_amdgcn_sched_barrier(0);
  const int v0 = tt * 384 + c8 * 16, v1 = v0 + 128, v2 = v0 + 256;
  const unsigned wbase = g * HB + (j * NWAVES + wave) * 6144;
  auto hoff = [&](unsigned m) -> unsigned { return (m >> 5) * (8u * HB) + ((m >> 1) & 15u) * ITB + (m & 1u) * 3072u + wbase; };
  auto mkr = [&](const uint16_t* b, unsigned n) { return __builtin_amdgcn_make_buffer_rsrc((void*)b, 0, n, 0x00020000); };
#define LDS_(S, rs, so) { S.d[0] = __builtin_amdgcn_raw_buffer_load_b128(rs, v0, (so), KVAUX); S.d[1] = __builtin_amdgcn_raw_buffer_load_b128(rs, v1, (so), KVAUX); S.d[2] = __builtin_amdgcn_raw_buffer_load_b128(rs, v2, (so), KVAUX); }
  Slot S0, S1, S2, S3, X0, X1, X2, X3;
  {
    const __amdgpu_buffer_rsrc_t r = mkr(p.k[0], BUFB);
    LDS_(X0, r, hoff(0)); LDS_(X1, r, hoff(1)); LDS_(X2, r, hoff(2)); LDS_(X3, r, hoff(3)); LDS_(S0, r, hoff(4)); LDS_(S1, r, hoff(5));
  }
  __builtin_amdgcn_sched_barrier(0);

  const unsigned long long epoch = ((unsigned long long)ep[1] << 32) | ep[0];
  unsigned long long z = epoch + 0x9E3779B97F4A7C15ull + 1ull;
  z = (z ^ (z >> 30)) * 0xBF58476D1CE4E5B9ull; z = (z ^ (z >> 27)) * 0x94D049BB133111EBull; z ^= z >> 31;
  const unsigned ta = (unsigned)z, tb = (unsigned)(z >> 32);

  if (qprod) {
    float a[8] = {0, 0, 0, 0, 0, 0, 0, 0};
    const unsigned xs[10] = {xa[0], xa[1], xa[2], xa[3], xb[0], xb[1], xb[2], xb[3], xc[0], xc[1]};
#pragma unroll
    for (int i = 0; i < 20; ++i) {
      const float xv = (i & 1) ? hi2f(xs[i >> 1]) : lo2f(xs[i >> 1]);
#pragma unroll
      for (int e = 0; e < 4; ++e) { a[2 * e] = fmaf(xv, lo2f(w[i][e]), a[2 * e]); a[2 * e + 1] = fmaf(xv, hi2f(w[i][e]), a[2 * e + 1]); }
    }
    float r4[4], r2[2];
#pragma unroll
    for (int e = 0; e < 4; ++e) r4[e] = swap32_sum(a[e], a[e + 4]);
#pragma unroll
    for (int e = 0; e < 2; ++e) r2[e] = swap16_sum(r4[e], r4[e + 2]);
    const float u0 = r2[0] + dpp<ROR8>(r2[0]), u1 = r2[1] + dpp<ROR8>(r2[1]);
    const int e = (lane >= 32 ? 4 : 0) + ((lane & 16) ? 2 : 0) + ((lane & 8) ? 1 : 0);
    sred[wave * 64 + c8 * 8 + e] = (lane & 8) ? u1 : u0;
  }
  __syncthreads();
  if (qprod && wave == 0) {
    const float s = (sred[lane] + sred[64 + lane]) + (sred[128 + lane] + sred[192 + lane]);
    const float b = __uint_as_float(f2bf(s));
    const unsigned b0 = __float_as_uint(dpp<0x00>(b)), b1 = __float_as_uint(dpp<0x55>(b)), b2 = __float_as_uint(dpp<0xAA>(b)), b3 = __float_as_uint(dpp<0xFF>(b));
    if ((lane & 3) == 0)
      __builtin_amdgcn_raw_buffer_store_b128(mkrec(b0 | (b1 << 16), b2 | (b3 << 16), ta, tb), rws, WS_Q + (g * 384 + j * 16 + (lane >> 2)) * 16, 0, AUX_SC1);
  }
  STAMP(1);
  {
    const int r0 = wave * 96 + lane, r1 = wave * 96 + 64 + (lane & 31);
    u32x4 a, b;
    for (unsigned spin = 0; spin < SPIN_CAP; ++spin) {
      asm volatile("" ::: "memory");
      a = __builtin_amdgcn_raw_buffer_load_b128(rws, WS_Q + (g * 384 + r0) * 16, 0, AUX_SC1);
      b = __builtin_amdgcn_raw_buffer_load_b128(rws, WS_Q + (g * 384 + r1) * 16, 0, AUX_SC1);
      if (__all(tags_ok(a, ta, tb) && tags_ok(b, ta, tb))) break;
      __builtin_amdgcn_s_sleep(2);
    }
    *(u32x2*)&sq[2 * r0] = u32x2{a[0], a[2]};
    *(u32x2*)&sq[2 * r1] = u32x2{b[0], b[2]};
  }
  __syncthreads();
  STAMP(2);

  constexpr float C2 = 0.07216878364870322f * 1.4426950408889634f;
  float* const sp = (float*)(smem + L_S) + wave * 1024;
  u32x4 qf[3];
  float mx;
  float acc[24];
  auto kstep = [&](const Slot& S, float* dst) {
    float s0 = 0.f, s1 = 0.f, s2 = 0.f;
#pragma unroll
    for (int e = 0; e < 4; ++e) { s0 = dot2(S.d[0][e], qf[0][e], s0); s1 = dot2(S.d[1][e], qf[1][e], s1); s2 = dot2(S.d[2][e], qf[2][e], s2); }
    float s = (s0 + s1) + s2;
    s += dpp<XOR1>(s); s += dpp<XOR2>(s); s += dpp<XOR7>(s);
    mx = fmaxf(mx, s);
    if (c8 == 0) *dst = s;
  };
  auto vstep = [&](const Slot& S, float pr) {
#pragma unroll
    for (int i = 0; i < 3; ++i)
#pragma unroll
      for (int e = 0; e < 4; ++e) {
        acc[i * 8 + 2 * e] = fmaf(pr, lo2f(S.d[i][e]), acc[i * 8 + 2 * e]);
        acc[i * 8 + 2 * e + 1] = fmaf(pr, hi2f(S.d[i][e]), acc[i * 8 + 2 * e + 1]);
      }
  };
#define SB() __builtin_amdgcn_sched_barrier(0)
#define WAITSLOT(on) { if (on) asm volatile("s_waitcnt vmcnt(3)" ::: "memory"); SB(); }
#define PIN_ACC() asm volatile("" : "+v"(acc[0]), "+v"(acc[1]), "+v"(acc[2]), "+v"(acc[3]), "+v"(acc[4]), "+v"(acc[5]), "+v"(acc[6]), "+v"(acc[7]), \
    "+v"(acc[8]), "+v"(acc[9]), "+v"(acc[10]), "+v"(acc[11]), "+v"(acc[12]), "+v"(acc[13]), "+v"(acc[14]), "+v"(acc[15]), \
    "+v"(acc[16]), "+v"(acc[17]), "+v"(acc[18]), "+v"(acc[19]), "+v"(acc[20]), "+v"(acc[21]), "+v"(acc[22]), "+v"(acc[23]) :: "memory")
  const int ths = j >> 2, tbr = ths >> 2, trow = (8 * (ths & 3) + g) * DH + (j & 3) * 48 + wave * 12;
  u32x4 wa[12]; unsigned wb[12];
  auto load_q = [&](int hs) {
#pragma unroll
    for (int i = 0; i < 3; ++i) qf[i] = *(const u32x4*)&sq[hs * 96 + 32 * i + 4 * c8];
    mx = -INFINITY;
  };
  auto kbody = [&](unsigned m, const __amdgpu_buffer_rsrc_t& rc, const __amdgpu_buffer_rsrc_t& rl) {
    float* dst = sp + m * 8 + tt;
#define KHALF(CUR, NXT, RS, OFF, DST) \
    if (KMODE == 0) { WAITSLOT(1); if (KSLEEP) __builtin_amdgcn_s_sleep(KSLEEP); LDS_(NXT, RS, OFF); SB(); kstep(CUR, DST); SB(); } \
    else { kstep(CUR, DST); SB(); if (KSLEEP) __builtin_amdgcn_s_sleep(KSLEEP); LDS_(NXT, RS, OFF); SB(); }
    KHALF(S0, S2, rc, hoff(m + 2), dst)
    KHALF(S1, S3, rc, hoff(m + 3), dst + 8)
    KHALF(S2, S0, rl, hoff((m + 4) & 127), dst + 16)
    KHALF(S3, S1, rl, hoff((m + 5) & 127), dst + 24)
  };
  auto khead_end = [&](int br, int hd) {
    float M = fmaxf(mx, dpp<ROR8>(mx)); M = swap16_max(M); M = swap32_max(M);
    const float Ms = M * C2;
    f32x4 s4 = *(const f32x4*)(sp + hd * 256 + lane * 4);
#pragma unroll
    for (int e = 0; e < 4; ++e) s4[e] = __builtin_amdgcn_exp2f(fmaf(s4[e], C2, -Ms));
    *(f32x4*)(sp + hd * 256 + lane * 4) = s4;
    float l = sum32((s4[0] + s4[1]) + (s4[2] + s4[3]));
    l = swap32_sum(l, l);
    if (lane == 0) { float* rec = sm + ((br * 4 + hd) * NWAVES + wave) * PREC; rec[0] = Ms; rec[1] = l; }
  };
  auto vbody = [&](unsigned m, const __amdgpu_buffer_rsrc_t& rc, const __amdgpu_buffer_rsrc_t& rl, bool last) {
    const float* src = sp + m * 8 + tt;
    const float p0 = src[0], p1 = src[8], p2 = src[16], p3 = src[24];
#define VHALF(CUR, NXT, RS, OFF, PR) \
    if (VMODE == 0) { WAITSLOT(1); if (VSLEEP) __builtin_amdgcn_s_sleep(VSLEEP); LDS_(NXT, RS, OFF); SB(); vstep(CUR, PR); PIN_ACC(); SB(); } \
    else { vstep(CUR, PR); PIN_ACC(); SB(); if (VSLEEP) __builtin_amdgcn_s_sleep(VSLEEP); LDS_(NXT, RS, OFF); SB(); }
    VHALF(S0, S2, rc, hoff(m + 2), p0)
    VHALF(S1, S3, rc, hoff(m + 3), p1)
    if (last) {
      const __amdgpu_buffer_rsrc_t rwo = __builtin_amdgcn_make_buffer_rsrc((void*)(p.wo[tbr] + (size_t)trow * D), 0, 12 * D * 2, 0x00020000);
#pragma unroll
      for (int r = 0; r < 12; ++r) {
        wa[r] = __builtin_amdgcn_raw_buffer_load_b128(rwo, lane * 20, r * D * 2, KVAUX);
        wb[r] = __builtin_amdgcn_raw_buffer_load_b32(rwo, lane * 20 + 16, r * D * 2, KVAUX);
      }
      SB(); vstep(S2, p2); PIN_ACC(); SB();
      vstep(S3, p3); PIN_ACC(); SB();
    } else {
      VHALF(S2, S0, rl, hoff((m + 4) & 127), p2)
      VHALF(S3, S1, rl, hoff((m + 5) & 127), p3)
    }
  };
  auto vflush = [&](int br, int hd) {
    float r12[12], r6[6], r3[3];
#pragma unroll
    for (int a = 0; a < 12; ++a) r12[a] = swap32_sum(acc[a], acc[a + 12]);
#pragma unroll
    for (int a = 0; a < 6; ++a) r6[a] = swap16_sum(r12[a], r12[a + 6]);
#pragma unroll
    for (int a = 0; a < 3; ++a) {
      const float u = r6[a] + dpp<ROR8>(r6[a]), v = r6[a + 3] + dpp<ROR8>(r6[a + 3]);
      r3[a] = (lane & 8) ? v : u;
    }
    float* rec = sm + ((br * 4 + hd) * NWAVES + wave) * PREC;
    const int abase = (lane >= 32 ? 12 : 0) + ((lane & 16) ? 6 : 0) + ((lane & 8) ? 3 : 0);
#pragma unroll
    for (int a = 0; a < 3; ++a) { const int ai = abase + a; rec[4 + 64 * (ai >> 3) + 8 * c8 + (ai & 7)] = r3[a]; }
#pragma unroll
    for (int a = 0; a < 24; ++a) acc[a] = 0.f;
  };
#pragma unroll
  for (int a = 0; a < 24; ++a) acc[a] = 0.f;

  load_q(0);
  kstep(X0, sp + tt); kstep(X1, sp + 8 + tt); kstep(X2, sp + 16 + tt); kstep(X3, sp + 24 + tt);
  SB();
#pragma unroll 1
  for (int br = 0; br < 2; ++br) {
    const uint16_t* kb = br ? p.k[1] : p.k[0];
    const uint16_t* vb = br ? p.v[1] : p.v[0];
    {
      const __amdgpu_buffer_rsrc_t rc = mkr(kb, BUFB), rn = mkr(vb, BUFB);
#pragma unroll 1
      for (int hd = 0; hd < 4; ++hd) {
        if (br + hd > 0) load_q(br * 4 + hd);
        const unsigned m0 = __builtin_amdgcn_readfirstlane(hd * 32 + ((br + hd == 0) ? 4 : 0));
#pragma unroll 1
        for (unsigned m = m0; m < (unsigned)(hd * 32 + 32); m += 4) {
          const __amdgpu_buffer_rsrc_t rl = m == 124 ? rn : rc;
          kbody(m, rc, rl);
        }
        khead_end(br, hd);
      }
    }
    if (br == 0) { STAMP(3); } else { STAMP(5); }
    {
      const __amdgpu_buffer_rsrc_t rc = mkr(vb, BUFB), rn = mkr(p.k[1], BUFB);
#pragma unroll 1
      for (int hd = 0; hd < 4 - br; ++hd) {
#pragma unroll 1
        for (int i = 0; i < 8; ++i) {
          const unsigned m = hd * 32 + i * 4;
          const __amdgpu_buffer_rsrc_t rl = m == 124 ? rn : rc;
          vbody(m, rc, rl, false);
        }
        vflush(br, hd);
      }
    }
    if (br == 0) { STAMP(4); }
  }
  {
    const __amdgpu_buffer_rsrc_t rc = mkr(p.v[1], BUFB);
#pragma unroll 1
    for (int i = 0; i < 7; ++i) vbody(96 + i * 4, rc, rc, false);
    vbody(124, rc, rc, true);
    vflush(1, 3);
  }
  STAMP(6);
  __syncthreads();
  STAMP(7);

#pragma unroll
  for (int i = 0; i < 4; ++i) {
    const int R = tid + 256 * i, hd = R >> 7, sl = (R >> 3) & 15, k = R & 7;
    const float* rec = sm + hd * NWAVES * PREC;
    const float m0 = rec[0], m1 = rec[PREC], m2 = rec[2 * PREC], m3 = rec[3 * PREC];
    const float M = fmaxf(fmaxf(m0, m1), fmaxf(m2, m3));
    const float e0 = __builtin_amdgcn_exp2f(m0 - M), e1 = __builtin_amdgcn_exp2f(m1 - M), e2 = __builtin_amdgcn_exp2f(m2 - M), e3 = __builtin_amdgcn_exp2f(m3 - M);
    const bool hdr = (k == 0 || k == 7);
    const int ia = hdr ? 1 : 4 + 12 * sl + 2 * (k - 1), ib = hdr ? 1 : ia + 1;
    const float va = (e0 * rec[ia] + e1 * rec[PREC + ia]) + (e2 * rec[2 * PREC + ia] + e3 * rec[3 * PREC + ia]);
    const float vb = (e0 * rec[ib] + e1 * rec[PREC + ib]) + (e2 * rec[2 * PREC + ib] + e3 * rec[3 * PREC + ib]);
    __builtin_amdgcn_raw_buffer_store_b128(mkrec(__float_as_uint(hdr ? M : va), __float_as_uint(vb), ta, tb), rws, WS_H1 + bid * 16384 + R * 16, 0, AUX_SC1);
  }
  float o6[3][2];
  {
    const int jj = lane & 31, half = lane >> 5;
    const int off = WS_H1 + (g * WPG + jj) * 16384 + ths * 2048 + ((j & 3) * 4 + wave) * 128 + half * 64;
    u32x4 r[4];
    for (unsigned spin = 0; spin < SPIN_CAP; ++spin) {
      asm volatile("" ::: "memory");
#pragma unroll
      for (int i = 0; i < 4; ++i) r[i] = __builtin_amdgcn_raw_buffer_load_b128(rws, off + 16 * i, 0, AUX_SC1);
      if (__all(tags_ok(r[0], ta, tb) && tags_ok(r[1], ta, tb) && tags_ok(r[2], ta, tb) && tags_ok(r[3], ta, tb))) break;
      __builtin_amdgcn_s_sleep(2);
    }
    const float mj = __uint_as_float(half ? r[3][0] : r[0][0]), lj = __uint_as_float(half ? r[3][2] : r[0][2]);
    const float M = max32(mj);
    const float wj = __builtin_amdgcn_exp2f(mj - M);
    const float inv = 1.f / sum32(wj * lj);
#pragma unroll
    for (int i = 0; i < 3; ++i) {
      const u32x4 q = half ? r[i] : r[i + 1];
      o6[i][0] = __uint_as_float(f2bf(sum32(wj * __uint_as_float(q[0])) * inv) << 16);
      o6[i][1] = __uint_as_float(f2bf(sum32(wj * __uint_as_float(q[2])) * inv) << 16);
    }
  }
  STAMP(8);
  {
    float y[10] = {0, 0, 0, 0, 0, 0, 0, 0, 0, 0};
#pragma unroll
    for (int r = 0; r < 12; ++r) {
      const float ov = __int_as_float(__builtin_amdgcn_readlane(__float_as_int(o6[(r % 6) >> 1][r & 1]), r < 6 ? 0 : 32));
#pragma unroll
      for (int e = 0; e < 4; ++e) { y[2 * e] = fmaf(ov, lo2f(wa[r][e]), y[2 * e]); y[2 * e + 1] = fmaf(ov, hi2f(wa[r][e]), y[2 * e + 1]); }
      y[8] = fmaf(ov, lo2f(wb[r]), y[8]); y[9] = fmaf(ov, hi2f(wb[r]), y[9]);
    }
#pragma unroll
    for (int e = 0; e < 5; ++e) *(float2*)&sred[wave * D + lane * 10 + 2 * e] = make_float2(y[2 * e], y[2 * e + 1]);
  }
  __syncthreads();
#pragma unroll
  for (int i = 0; i < 2; ++i) {
    const int n = tid + 256 * i;
    if (n < 320) {
      const float a = (sred[2 * n] + sred[D + 2 * n]) + (sred[2 * D + 2 * n] + sred[3 * D + 2 * n]);
      const float b = (sred[2 * n + 1] + sred[D + 2 * n + 1]) + (sred[2 * D + 2 * n + 1] + sred[3 * D + 2 * n + 1]);
      __builtin_amdgcn_raw_buffer_store_b128(mkrec(__float_as_uint(a), __float_as_uint(b), ta, tb), rws, WS_H2 + (tbr * 128 + g * 16 + (j & 15)) * 5120 + n * 16, 0, AUX_SC1);
    }
  }
  STAMP(9);
  if ((j & 15) < 5) {
    const int c = g * 5 + (j & 15);
    const int pp = wave * 32 + (lane & 31), half = lane >> 5;
    const int off = WS_H2 + (tbr * 128 + pp) * 5120 + (8 * c + 4 * half) * 16;
    u32x4 r[4];
    for (unsigned spin = 0; spin < SPIN_CAP; ++spin) {
      asm volatile("" ::: "memory");
#pragma unroll
      for (int i = 0; i < 4; ++i) r[i] = __builtin_amdgcn_raw_buffer_load_b128(rws, off + 16 * i, 0, AUX_SC1);
      if (__all(tags_ok(r[0], ta, tb) && tags_ok(r[1], ta, tb) && tags_ok(r[2], ta, tb) && tags_ok(r[3], ta, tb))) break;
      __builtin_amdgcn_s_sleep(2);
    }
    float* fin = sm;
#pragma unroll
    for (int i = 0; i < 4; ++i) {
      const float s0 = sum32(__uint_as_float(r[i][0])), s1 = sum32(__uint_as_float(r[i][2]));
      if ((lane & 31) == 0) { fin[wave * 16 + half * 8 + 2 * i] = s0; fin[wave * 16 + half * 8 + 2 * i + 1] = s1; }
    }
    __syncthreads();
    if (tid < 8) {
      const float a = (fin[2 * tid] + fin[16 + 2 * tid]) + (fin[32 + 2 * tid] + fin[48 + 2 * tid]);
      const float cc = (fin[2 * tid + 1] + fin[16 + 2 * tid + 1]) + (fin[32 + 2 * tid + 1] + fin[48 + 2 * tid + 1]);
      ((unsigned*)p.out)[(tbr * D + 16 * c) / 2 + tid] = f2bf(a) | (f2bf(cc) << 16);
    }
    if (bid == 0 && tid == 0) __hip_atomic_store((GAS unsigned long long*)(p.ws + WS_EPOCH), epoch + 1ull, __ATOMIC_RELAXED, __HIP_MEMORY_SCOPE_AGENT);
  }
}

extern "C" void kernel_launch(void* const* d_in, const int* in_sizes, int n_in, void* d_out, int out_size, void* d_ws, size_t ws_size, hipStream_t stream) {
  Params p;
  for (int b = 0; b < 2; ++b) {
    p.x[b] = (const uint16_t*)d_in[5 * b + 0];
    p.k[b] = (const uint16_t*)d_in[5 * b + 1];
    p.v[b] = (const uint16_t*)d_in[5 * b + 2];
    p.wq[b] = (const uint16_t*)d_in[5 * b + 3];
    p.wo[b] = (const uint16_t*)d_in[5 * b + 4];
  }
  p.out = (uint16_t*)d_out;
  p.ws = (unsigned char*)d_ws;
  (void)hipFuncSetAttribute((const void*)k_fused, hipFuncAttributeMaxDynamicSharedMemorySize, LDS_BYTES);
  k_fused<<<NWG, NTHR, LDS_BYTES, stream>>>(p);
}
```

```cpp
#include <hip/hip_runtime.h>
#include <hip/hip_bf16.h>
#include <stdint.h>

typedef unsigned u32x4 __attribute__((ext_vector_type(4)));
typedef unsigned u32x2 __attribute__((ext_vector_type(2)));
typedef unsigned u32x4a8 __attribute__((ext_vector_type(4), aligned(8)));
typedef float f32x4 __attribute__((ext_vector_type(4)));
typedef __bf16 bf16x2 __attribute__((ext_vector_type(2)));
#define GAS __attribute__((address_space(1)))
#define LAS __attribute__((address_space(3)))
#define RLX_WG __ATOMIC_RELAXED, __HIP_MEMORY_SCOPE_WORKGROUP

constexpr int D = 640, SK = 32768, DH = 192, HD = 32 * DH;
constexpr int NWG = 256, NWAVES = 4, NTHR = 320;
constexpr int WPG = 32;
constexpr unsigned HB = SK * DH * 2;
constexpr unsigned BUFB = 32u * HB;
constexpr unsigned UNITB = 128u * DH * 2u;
constexpr int NB = 32;
#define KSL 3
#define VSL 3
#define PPER 4
#define PSLOW 0
#define SGAIN 45
#define PGAIN 45
#define PDEAD 10
#define PACE 1
#define GMAP 1
#define SKEW 0
#define YPRE 0
#define KVAUX 2
constexpr int AUX_SC1 = 16;
constexpr unsigned SPIN_CAP = 1u << 20;

constexpr int WS_EPOCH = 0;
constexpr int WS_PACE = 6 << 20;
constexpr int WS_Q = 4096;
constexpr int WS_H1 = 65536;
constexpr int WS_H2 = WS_H1 + NWG * 16384;
constexpr int WS_END = WS_PACE + 64 * 8 * 32 * 8;
static_assert(WS_H2 + NWG * 5120 <= WS_PACE, "map");

constexpr int PREC = 196;
constexpr int L_REC = 0;
constexpr int L_Q = L_REC + 8 * NWAVES * PREC * 4;
constexpr int L_S = L_Q + 8 * 96 * 4;
constexpr int L_RED = L_S + NWAVES * NB * 32 * 4;
constexpr int L_QR = L_RED + NWAVES * D * 4;
constexpr int L_FIN = L_QR + 256 * 4;
constexpr int L_PACE = L_FIN + 64 * 4;
constexpr int L_TR = L_PACE + 16;
constexpr int L_END = L_TR + 4 * 128 * 4;
constexpr int LDS_BYTES = 84 * 1024;
static_assert(L_END <= LDS_BYTES, "lds");

struct Params {
  const uint16_t* x[2];
  const uint16_t* k[2];
  const uint16_t* v[2];
  const uint16_t* wq[2];
  const uint16_t* wo[2];
  uint16_t* out;
  unsigned char* ws;
};

__device__ __forceinline__ float lo2f(unsigned u) { return __uint_as_float(u << 16); }
__device__ __forceinline__ float hi2f(unsigned u) { return __uint_as_float(u & 0xffff0000u); }
__device__ __forceinline__ unsigned f2bf(float f) {
  unsigned u = __float_as_uint(f);
  u += 0x7FFFu + ((u >> 16) & 1u);
  return u >> 16;
}
__device__ __forceinline__ float dot2(unsigned k, unsigned q, float acc) {
  return __builtin_amdgcn_fdot2_f32_bf16(__builtin_bit_cast(bf16x2, k), __builtin_bit_cast(bf16x2, q), acc, false);
}
template <int CTRL> __device__ __forceinline__ float dpp(float x) {
  return __int_as_float(__builtin_amdgcn_mov_dpp(__float_as_int(x), CTRL, 0xf, 0xf, true));
}
constexpr int XOR1 = 0xB1, XOR2 = 0x4E, XOR7 = 0x141, ROR8 = 0x128;
__device__ __forceinline__ float swap32_sum(float a, float b) {
  auto s = __builtin_amdgcn_permlane32_swap(__float_as_uint(a), __float_as_uint(b), false, false);
  return __uint_as_float(s[0]) + __uint_as_float(s[1]);
}
__device__ __forceinline__ float swap16_sum(float a, float b) {
  auto s = __builtin_amdgcn_permlane16_swap(__float_as_uint(a), __float_as_uint(b), false, false);
  return __uint_as_float(s[0]) + __uint_as_float(s[1]);
}
__device__ __forceinline__ float swap32_max(float a) {
  auto s = __builtin_amdgcn_permlane32_swap(__float_as_uint(a), __float_as_uint(a), false, false);
  return fmaxf(__uint_as_float(s[0]), __uint_as_float(s[1]));
}
__device__ __forceinline__ float swap16_max(float a) {
  auto s = __builtin_amdgcn_permlane16_swap(__float_as_uint(a), __float_as_uint(a), false, false);
  return fmaxf(__uint_as_float(s[0]), __uint_as_float(s[1]));
}
__device__ __forceinline__ float sum32(float x) {
  x += dpp<XOR1>(x); x += dpp<XOR2>(x); x += dpp<XOR7>(x); x += dpp<ROR8>(x);
  return swap16_sum(x, x);
}
__device__ __forceinline__ float max32(float x) {
  x = fmaxf(x, dpp<XOR1>(x)); x = fmaxf(x, dpp<XOR2>(x)); x = fmaxf(x, dpp<XOR7>(x)); x = fmaxf(x, dpp<ROR8>(x));
  return swap16_max(x);
}
__device__ __forceinline__ bool tags_ok(const u32x4& r, unsigned ta, unsigned tb) { return r[1] == ta && r[3] == tb; }
__device__ __forceinline__ u32x4 mkrec(unsigned a, unsigned b, unsigned ta, unsigned tb) { return u32x4{a, ta, b, tb}; }

struct Slot { u32x4 d[3]; };

__global__ void __launch_bounds__(NTHR, 2) k_fused(Params p) {
  extern __shared__ __attribute__((aligned(16))) unsigned char smem[];
  float* const sm = (float*)(smem + L_REC);
  unsigned* const sq = (unsigned*)(smem + L_Q);
  float* const sred = (float*)(smem + L_RED);
  float* const sqr = (float*)(smem + L_QR);
  float* const fin = (float*)(smem + L_FIN);

  const int bid = blockIdx.x, g = GMAP ? bid % 8 : bid / WPG, j = GMAP ? bid / 8 : bid % WPG;
  const int tid = threadIdx.x, lane = tid & 63, wave = __builtin_amdgcn_readfirstlane(tid >> 6);
  const int c8 = lane & 7, tt = lane >> 3;
#define STAMP(i)
#define TRACE(idx)
  STAMP(0);

  LAS int* const pace = (LAS int*)(smem + L_PACE);
  if (wave == NWAVES) {
    if (lane == 0) { pace[0] = -1; pace[1] = 0; pace[2] = 0; }
    const __amdgpu_buffer_rsrc_t rw = __builtin_amdgcn_make_buffer_rsrc((void*)p.ws, 0, WS_END, 0x00020000);
    const u32x2 e2 = __builtin_amdgcn_raw_buffer_load_b64(rw, WS_EPOCH, 0, AUX_SC1);
    unsigned long long z2 = (((unsigned long long)e2[1] << 32) | e2[0]) + 0x9E3779B97F4A7C15ull + 1ull;
    z2 = (z2 ^ (z2 >> 30)) * 0xBF58476D1CE4E5B9ull; z2 = (z2 ^ (z2 >> 27)) * 0x94D049BB133111EBull; z2 ^= z2 >> 31;
    const unsigned tag = (unsigned)z2;
    asm volatile("s_waitcnt vmcnt(0) lgkmcnt(0)" ::: "memory");
    __builtin_amdgcn_s_barrier();
    __builtin_amdgcn_s_barrier();
    const int slot = (j & 7) * 32 + g * 4 + (j >> 3);
    unsigned tprev = 0; int kprev = 0, sprev = 0;
    for (int c = 1; c < 128 / PPER; ++c) {
      for (unsigned spin = 0; spin < SPIN_CAP && __hip_atomic_load(pace, RLX_WG) < PPER * c; ++spin) __builtin_amdgcn_s_sleep(4);
      const unsigned now = (unsigned)__builtin_amdgcn_s_memrealtime();
      if (lane == 0) __builtin_amdgcn_raw_buffer_store_b64(u32x2{now, tag}, rw, WS_PACE + (c * 256 + slot) * 8, 0, AUX_SC1);
      if (PACE && c > 1) {
        const u32x2 pr = __builtin_amdgcn_raw_buffer_load_b64(rw, WS_PACE + ((c - 1) * 256 + (j & 7) * 32 + (lane & 31)) * 8, 0, AUX_SC1);
        const float d = (float)(int)((pr[1] == tag ? pr[0] : now) - tprev);
        const float mean = sum32(d) * (1.f / 32.f);
        const int pred = (int)(-mean) - PGAIN * kprev + SGAIN * sprev;
        int k = (pred - PDEAD) / PGAIN, sl = PSLOW ? (-pred - PDEAD) / SGAIN : 0;
        k = __builtin_amdgcn_readfirstlane(k < 0 ? 0 : k > PPER - 1 ? PPER - 1 : k);
        sl = __builtin_amdgcn_readfirstlane(sl < 0 ? 0 : sl > PPER - 1 ? PPER - 1 : sl);
        if (lane == 0) { __hip_atomic_store(pace + 1, PPER * c + 2 + k, RLX_WG); __hip_atomic_store(pace + 2, PPER * c + 2 + sl, RLX_WG); }
        kprev = k; sprev = sl;
      }
      tprev = now;
    }
    return;
  }

  const __amdgpu_buffer_rsrc_t rws = __builtin_amdgcn_make_buffer_rsrc((void*)p.ws, 0, WS_END, 0x00020000);
  int eoff;
  asm volatile("v_mov_b32 %0, %1" : "=v"(eoff) : "s"(WS_EPOCH));
  const u32x2 ep = __builtin_amdgcn_raw_buffer_load_b64(rws, eoff, 0, AUX_SC1);

  u32x4 w[20];
  u32x4 xa, xb; u32x2 xc;
  const bool qprod = j < 24;
  if (qprod) {
    const int hs = j / 3, part = j % 3, qb = hs >> 2, qh = 8 * (hs & 3) + g;
    const int rr = tid >> 3;
    const uint16_t* x = p.x[qb] + rr * 20;
    xa = *(const GAS u32x4a8*)(x);
    xb = *(const GAS u32x4a8*)(x + 8);
    xc = *(const GAS u32x2*)(x + 16);
    __builtin_amdgcn_sched_barrier(0);
    const uint16_t* wq = p.wq[qb] + (size_t)(rr * 20) * HD + qh * DH + part * 64 + c8 * 8;
#pragma unroll
    for (int i = 0; i < 20; ++i) w[i] = __builtin_nontemporal_load((const GAS u32x4*)(wq + (size_t)i * HD));
  }
  __builtin_amdgcn_sched_barrier(0);
  const int v0 = tt * 384 + c8 * 16, v1 = v0 + 128, v2 = v0 + 256;
  constexpr unsigned HS1 = 3072, HS2 = 6144, HS3 = 9216;
  const unsigned wbase = g * HB + j * UNITB + wave * 12288;
  auto uoff = [&](unsigned n) -> unsigned { return (n >> 3) * (8u * HB) + ((n + SKEW * g) & 7u) * (32u * UNITB) + wbase; };
  auto mkr = [&](const uint16_t* b, unsigned n) { return __builtin_amdgcn_make_buffer_rsrc((void*)b, 0, n, 0x00020000); };
#define LDS_(S, rs, so) { S.d[0] = __builtin_amdgcn_raw_buffer_load_b128(rs, v0, (so), KVAUX); S.d[1] = __builtin_amdgcn_raw_buffer_load_b128(rs, v1, (so), KVAUX); S.d[2] = __builtin_amdgcn_raw_buffer_load_b128(rs, v2, (so), KVAUX); }
  Slot S0, S1, S2, S3, X0, X1, X2, X3;
  const __amdgpu_buffer_rsrc_t rk0 = mkr(p.k[0], BUFB);
  {
    const unsigned o0 = uoff(0);
    LDS_(X0, rk0, o0); LDS_(X1, rk0, o0 + HS1); LDS_(X2, rk0, o0 + HS2); LDS_(X3, rk0, o0 + HS3);
    LDS_(S0, rk0, uoff(1)); LDS_(S1, rk0, uoff(1) + HS1);
  }
  __builtin_amdgcn_sched_barrier(0);

  if (qprod) {
    float a[8] = {0, 0, 0, 0, 0, 0, 0, 0};
    const unsigned xs[10] = {xa[0], xa[1], xa[2], xa[3], xb[0], xb[1], xb[2], xb[3], xc[0], xc[1]};
#pragma unroll
    for (int i = 0; i < 20; ++i) {
      const unsigned xlo = (i & 1) ? xs[i >> 1] >> 16 : xs[i >> 1] & 0xffffu, xhi = xlo << 16;
#pragma unroll
      for (int e = 0; e < 4; ++e) { a[2 * e] = dot2(w[i][e], xlo, a[2 * e]); a[2 * e + 1] = dot2(w[i][e], xhi, a[2 * e + 1]); }
    }
    float r4[4], r2[2];
#pragma unroll
    for (int e = 0; e < 4; ++e) r4[e] = swap32_sum(a[e], a[e + 4]);
#pragma unroll
    for (int e = 0; e < 2; ++e) r2[e] = swap16_sum(r4[e], r4[e + 2]);
    const float u0 = r2[0] + dpp<ROR8>(r2[0]), u1 = r2[1] + dpp<ROR8>(r2[1]);
    const int e = (lane >= 32 ? 4 : 0) + ((lane & 16) ? 2 : 0) + ((lane & 8) ? 1 : 0);
    sqr[wave * 64 + c8 * 8 + e] = (lane & 8) ? u1 : u0;
  }
  const unsigned long long epoch = ((unsigned long long)ep[1] << 32) | ep[0];
  unsigned long long z = epoch + 0x9E3779B97F4A7C15ull + 1ull;
  z = (z ^ (z >> 30)) * 0xBF58476D1CE4E5B9ull; z = (z ^ (z >> 27)) * 0x94D049BB133111EBull; z ^= z >> 31;
  const unsigned ta = (unsigned)z, tb = (unsigned)(z >> 32);
  __syncthreads();
  if (qprod && wave == 0) {
    const float s = (sqr[lane] + sqr[64 + lane]) + (sqr[128 + lane] + sqr[192 + lane]);
    const float b = __uint_as_float(f2bf(s));
    const unsigned b0 = __float_as_uint(dpp<0x00>(b)), b1 = __float_as_uint(dpp<0x55>(b)), b2 = __float_as_uint(dpp<0xAA>(b)), b3 = __float_as_uint(dpp<0xFF>(b));
    if ((lane & 3) == 0)
      __builtin_amdgcn_raw_buffer_store_b128(mkrec(b0 | (b1 << 16), b2 | (b3 << 16), ta, tb), rws, WS_Q + (g * 384 + j * 16 + (lane >> 2)) * 16, 0, AUX_SC1);
  }
  STAMP(1);
  {
    const int r0 = wave * 96 + lane, r1 = wave * 96 + 64 + (lane & 31);
    u32x4 a, b;
    for (unsigned spin = 0; spin < SPIN_CAP; ++spin) {
      asm volatile("" ::: "memory");
      a = __builtin_amdgcn_raw_buffer_load_b128(rws, WS_Q + (g * 384 + r0) * 16, 0, AUX_SC1);
      b = __builtin_amdgcn_raw_buffer_load_b128(rws, WS_Q + (g * 384 + r1) * 16, 0, AUX_SC1);
      if (__all(tags_ok(a, ta, tb) && tags_ok(b, ta, tb))) break;
      __builtin_amdgcn_s_sleep(2);
    }
    *(u32x2*)&sq[2 * r0] = u32x2{a[0], a[2]};
    *(u32x2*)&sq[2 * r1] = u32x2{b[0], b[2]};
  }
  __syncthreads();
  STAMP(2);

  constexpr float C2 = 0.07216878364870322f * 1.4426950408889634f;
  float* const sp = (float*)(smem + L_S) + wave * (NB * 32);
  u32x4 qf[3];
  float mx, lsum;
  float acc[24];
  auto kstep = [&](const Slot& S, float* dst) {
    float s0 = 0.f, s1 = 0.f, s2 = 0.f;
#pragma unroll
    for (int e = 0; e < 4; ++e) { s0 = dot2(S.d[0][e], qf[0][e], s0); s1 = dot2(S.d[1][e], qf[1][e], s1); s2 = dot2(S.d[2][e], qf[2][e], s2); }
    float s = (s0 + s1) + s2;
    s += dpp<XOR1>(s); s += dpp<XOR2>(s); s += dpp<XOR7>(s);
    mx = fmaxf(mx, s);
    if (c8 == 0) *dst = s;
  };
  auto vstep = [&](const Slot& S, float pr) {
#pragma unroll
    for (int i = 0; i < 3; ++i)
#pragma unroll
      for (int e = 0; e < 4; ++e) {
        acc[i * 8 + 2 * e] = fmaf(pr, lo2f(S.d[i][e]), acc[i * 8 + 2 * e]);
        acc[i * 8 + 2 * e + 1] = fmaf(pr, hi2f(S.d[i][e]), acc[i * 8 + 2 * e + 1]);
      }
  };
#define SB() __builtin_amdgcn_sched_barrier(0)
#define PIN_ACC() asm volatile("" : "+v"(acc[0]), "+v"(acc[1]), "+v"(acc[2]), "+v"(acc[3]), "+v"(acc[4]), "+v"(acc[5]), "+v"(acc[6]), "+v"(acc[7]), \
    "+v"(acc[8]), "+v"(acc[9]), "+v"(acc[10]), "+v"(acc[11]), "+v"(acc[12]), "+v"(acc[13]), "+v"(acc[14]), "+v"(acc[15]), \
    "+v"(acc[16]), "+v"(acc[17]), "+v"(acc[18]), "+v"(acc[19]), "+v"(acc[20]), "+v"(acc[21]), "+v"(acc[22]), "+v"(acc[23]) :: "memory")
#define PACE_WAIT(SL) { if (!deep) { asm volatile("s_waitcnt vmcnt(3)" ::: "memory"); if (SL) __builtin_amdgcn_s_sleep(SL); if (PSLOW && slow) __builtin_amdgcn_s_sleep(PSLOW); } SB(); }
#define KHALF(CUR, NXT, RS, OFF, DST) { PACE_WAIT(KSL) LDS_(NXT, RS, OFF); SB(); kstep(CUR, DST); SB(); }
#define VHALF(CUR, NXT, RS, OFF, PR) { PACE_WAIT(VSL) LDS_(NXT, RS, OFF); SB(); vstep(CUR, PR); PIN_ACC(); SB(); }
  int pw_deep = 0, pw_slow = 0;
  auto pace_body = [&](int ng, bool& deep, bool& slow) {
    deep = ng < __builtin_amdgcn_readfirstlane(pw_deep);
    slow = ng < __builtin_amdgcn_readfirstlane(pw_slow);
    if (wave == 0 && lane == 0) __hip_atomic_store(pace, ng, RLX_WG);
    pw_deep = __hip_atomic_load(pace + 1, RLX_WG);
    pw_slow = __hip_atomic_load(pace + 2, RLX_WG);
  };
  const int ths = j >> 2, tbr = ths >> 2, trow = (8 * (ths & 3) + g) * DH + (j & 3) * 48 + wave * 12;
  u32x4 wa[12]; unsigned wb[12];
  auto load_q = [&](int hs) {
#pragma unroll
    for (int i = 0; i < 3; ++i) qf[i] = *(const u32x4*)&sq[hs * 96 + 32 * i + 4 * c8];
    mx = -INFINITY;
  };
  auto khead_end = [&](int hs) {
    float M = fmaxf(mx, dpp<ROR8>(mx)); M = swap16_max(M); M = swap32_max(M);
    if (lane == 0) sm[(hs * NWAVES + wave) * PREC] = M * C2;
  };
  auto vflush = [&](int hs) {
    float r12[12], r6[6], r3[3];
#pragma unroll
    for (int a = 0; a < 12; ++a) r12[a] = swap32_sum(acc[a], acc[a + 12]);
#pragma unroll
    for (int a = 0; a < 6; ++a) r6[a] = swap16_sum(r12[a], r12[a + 6]);
#pragma unroll
    for (int a = 0; a < 3; ++a) {
      const float u = r6[a] + dpp<ROR8>(r6[a]), v = r6[a + 3] + dpp<ROR8>(r6[a + 3]);
      r3[a] = (lane & 8) ? v : u;
    }
    float l = lsum + dpp<ROR8>(lsum);
    l = swap16_sum(l, l); l = swap32_sum(l, l);
    float* rec = sm + (hs * NWAVES + wave) * PREC;
    const int abase = (lane >= 32 ? 12 : 0) + ((lane & 16) ? 6 : 0) + ((lane & 8) ? 3 : 0);
#pragma unroll
    for (int a = 0; a < 3; ++a) { const int ai = abase + a; rec[4 + 64 * (ai >> 3) + 8 * c8 + (ai & 7)] = r3[a]; }
    if (lane == 0) rec[1] = l;
#pragma unroll
    for (int a = 0; a < 24; ++a) acc[a] = 0.f;
    lsum = 0.f;
  };
  auto probs = [&](int n, float Ms, float& p0, float& p1, float& p2, float& p3) {
    const float* src = sp + n * 32 + tt;
    p0 = __builtin_amdgcn_exp2f(fmaf(src[0], C2, -Ms)); p1 = __builtin_amdgcn_exp2f(fmaf(src[8], C2, -Ms));
    p2 = __builtin_amdgcn_exp2f(fmaf(src[16], C2, -Ms)); p3 = __builtin_amdgcn_exp2f(fmaf(src[24], C2, -Ms));
    lsum += (p0 + p1) + (p2 + p3);
  };
#pragma unroll
  for (int a = 0; a < 24; ++a) acc[a] = 0.f;
  lsum = 0.f;

  TRACE(0);
  load_q(0);
  kstep(X0, sp + tt); kstep(X1, sp + 8 + tt); kstep(X2, sp + 16 + tt); kstep(X3, sp + 24 + tt);
  SB();
  TRACE(127);
  float Ms = 0.f;
#pragma unroll 1
  for (int br = 0; br < 2; ++br) {
    const uint16_t* kb = br ? p.k[1] : p.k[0];
    const uint16_t* vb = br ? p.v[1] : p.v[0];
    {
      const __amdgpu_buffer_rsrc_t rc = mkr(kb, BUFB), rn = mkr(vb, BUFB);
      const int n0 = __builtin_amdgcn_readfirstlane(br ? 0 : 1 + YPRE);
#pragma unroll 1
      for (int n = n0; n < NB; ++n) {
        TRACE(br * 64 + n);
        if ((n & 7) == 0) { if (n) khead_end(br * 4 + (n >> 3) - 1); load_q(br * 4 + (n >> 3)); }
        float* dst = sp + n * 32 + tt;
        const unsigned o = uoff(n);
        const bool last = n == NB - 1;
        const __amdgpu_buffer_rsrc_t rl = last ? rn : rc;
        const unsigned on = uoff(last ? 0 : n + 1);
        bool deep, slow;
        pace_body(br * 64 + n, deep, slow);
        KHALF(S0, S2, rc, o + HS2, dst)
        KHALF(S1, S3, rc, o + HS3, dst + 8)
        KHALF(S2, S0, rl, on, dst + 16)
        KHALF(S3, S1, rl, on + HS1, dst + 24)
      }
      khead_end(br * 4 + 3);
    }
    if (br == 0) { STAMP(3); } else { STAMP(5); }
    {
      const __amdgpu_buffer_rsrc_t rc = mkr(vb, BUFB), rn = mkr(p.k[1], BUFB);
      const int nend = br ? NB - 1 : NB;
#pragma unroll 1
      for (int n = 0; n < nend; ++n) {
        TRACE(br * 64 + 32 + n);
        if ((n & 7) == 0) { if (n) vflush(br * 4 + (n >> 3) - 1); Ms = sm[((br * 4 + (n >> 3)) * NWAVES + wave) * PREC]; }
        float p0, p1, p2, p3;
        probs(n, Ms, p0, p1, p2, p3);
        const bool last = n == NB - 1;
        const __amdgpu_buffer_rsrc_t rl = last ? rn : rc;
        const unsigned o = uoff(n), on = uoff(last ? 0 : n + 1);
        bool deep, slow;
        pace_body(br * 64 + 32 + n, deep, slow);
        VHALF(S0, S2, rc, o + HS2, p0)
        VHALF(S1, S3, rc, o + HS3, p1)
        VHALF(S2, S0, rl, on, p2)
        VHALF(S3, S1, rl, on + HS1, p3)
      }
      if (br == 0) { vflush(3); STAMP(4); }
    }
  }
  {
    const __amdgpu_buffer_rsrc_t rc = mkr(p.v[1], BUFB);
    if (((NB - 1) & 7) == 0) { vflush(4 + ((NB - 1) >> 3) - 1); Ms = sm[((4 + ((NB - 1) >> 3)) * NWAVES + wave) * PREC]; }
    float p0, p1, p2, p3;
    probs(NB - 1, Ms, p0, p1, p2, p3);
    const unsigned o = uoff(NB - 1);
    LDS_(S2, rc, o + HS2); LDS_(S3, rc, o + HS3); SB();
    const __amdgpu_buffer_rsrc_t rwo = __builtin_amdgcn_make_buffer_rsrc((void*)(p.wo[tbr] + (size_t)trow * D), 0, 12 * D * 2, 0x00020000);
#pragma unroll
    for (int r = 0; r < 12; ++r) {
      wa[r] = __builtin_amdgcn_raw_buffer_load_b128(rwo, lane * 20, r * D * 2, KVAUX);
      wb[r] = __builtin_amdgcn_raw_buffer_load_b32(rwo, lane * 20 + 16, r * D * 2, KVAUX);
    }
    SB(); vstep(S0, p0); PIN_ACC(); SB();
    vstep(S1, p1); PIN_ACC(); SB();
    vstep(S2, p2); PIN_ACC(); SB();
    vstep(S3, p3); PIN_ACC(); SB();
    vflush(7);
  }
  STAMP(6);
  __syncthreads();
  STAMP(7);

#pragma unroll
  for (int i = 0; i < 4; ++i) {
    const int R = tid + 256 * i, hd = R >> 7, sl = (R >> 3) & 15, k = R & 7;
    const float* rec = sm + hd * NWAVES * PREC;
    const float m0 = rec[0], m1 = rec[PREC], m2 = rec[2 * PREC], m3 = rec[3 * PREC];
    const float M = fmaxf(fmaxf(m0, m1), fmaxf(m2, m3));
    const float e0 = __builtin_amdgcn_exp2f(m0 - M), e1 = __builtin_amdgcn_exp2f(m1 - M), e2 = __builtin_amdgcn_exp2f(m2 - M), e3 = __builtin_amdgcn_exp2f(m3 - M);
    const bool hdr = (k == 0 || k == 7);
    const int ia = hdr ? 1 : 4 + 12 * sl + 2 * (k - 1), ib = hdr ? 1 : ia + 1;
    const float va = (e0 * rec[ia] + e1 * rec[PREC + ia]) + (e2 * rec[2 * PREC + ia] + e3 * rec[3 * PREC + ia]);
    const float vb = (e0 * rec[ib] + e1 * rec[PREC + ib]) + (e2 * rec[2 * PREC + ib] + e3 * rec[3 * PREC + ib]);
    __builtin_amdgcn_raw_buffer_store_b128(mkrec(__float_as_uint(hdr ? M : va), __float_as_uint(vb), ta, tb), rws, WS_H1 + (g * WPG + j) * 16384 + R * 16, 0, AUX_SC1);
  }
  unsigned o6[3][2];
  {
    const int jj = lane & 31, half = lane >> 5;
    const int off = WS_H1 + (g * WPG + jj) * 16384 + ths * 2048 + ((j & 3) * 4 + wave) * 128 + half * 64;
    u32x4 r[4];
    for (unsigned spin = 0; spin < SPIN_CAP; ++spin) {
      asm volatile("" ::: "memory");
#pragma unroll
      for (int i = 0; i < 4; ++i) r[i] = __builtin_amdgcn_raw_buffer_load_b128(rws, off + 16 * i, 0, AUX_SC1);
      if (__all(tags_ok(r[0], ta, tb) && tags_ok(r[1], ta, tb) && tags_ok(r[2], ta, tb) && tags_ok(r[3], ta, tb))) break;
      __builtin_amdgcn_s_sleep(2);
    }
    const float mj = __uint_as_float(half ? r[3][0] : r[0][0]), lj = __uint_as_float(half ? r[3][2] : r[0][2]);
    const float M = max32(mj);
    const float wj = __builtin_amdgcn_exp2f(mj - M);
    const float inv = 1.f / sum32(wj * lj);
#pragma unroll
    for (int i = 0; i < 3; ++i) {
      const u32x4 q = half ? r[i] : r[i + 1];
      o6[i][0] = f2bf(sum32(wj * __uint_as_float(q[0])) * inv);
      o6[i][1] = f2bf(sum32(wj * __uint_as_float(q[2])) * inv);
    }
  }
  STAMP(8);
  {
    float y[10] = {0, 0, 0, 0, 0, 0, 0, 0, 0, 0};
#pragma unroll
    for (int r = 0; r < 12; ++r) {
      const unsigned olo = (unsigned)__builtin_amdgcn_readlane((int)o6[(r % 6) >> 1][r & 1], r < 6 ? 0 : 32), ohi = olo << 16;
#pragma unroll
      for (int e = 0; e < 4; ++e) { y[2 * e] = dot2(wa[r][e], olo, y[2 * e]); y[2 * e + 1] = dot2(wa[r][e], ohi, y[2 * e + 1]); }
      y[8] = dot2(wb[r], olo, y[8]); y[9] = dot2(wb[r], ohi, y[9]);
    }
#pragma unroll
    for (int e = 0; e < 5; ++e) *(float2*)&sred[wave * D + lane * 10 + 2 * e] = make_float2(y[2 * e], y[2 * e + 1]);
  }
  __syncthreads();
#pragma unroll
  for (int i = 0; i < 2; ++i) {
    const int n = tid + 256 * i;
    if (n < 320) {
      const float a = (sred[2 * n] + sred[D + 2 * n]) + (sred[2 * D + 2 * n] + sred[3 * D + 2 * n]);
      const float b = (sred[2 * n + 1] + sred[D + 2 * n + 1]) + (sred[2 * D + 2 * n + 1] + sred[3 * D + 2 * n + 1]);
      __builtin_amdgcn_raw_buffer_store_b128(mkrec(__float_as_uint(a), __float_as_uint(b), ta, tb), rws, WS_H2 + (tbr * 128 + g * 16 + (j & 15)) * 5120 + n * 16, 0, AUX_SC1);
    }
  }
  STAMP(9);
  if ((j & 15) < 5) {
    const int c = g * 5 + (j & 15);
    const int pp = wave * 32 + (lane & 31), half = lane >> 5;
    const int off = WS_H2 + (tbr * 128 + pp) * 5120 + (8 * c + 4 * half) * 16;
    u32x4 r[4];
    for (unsigned spin = 0; spin < SPIN_CAP; ++spin) {
      asm volatile("" ::: "memory");
#pragma unroll
      for (int i = 0; i < 4; ++i) r[i] = __builtin_amdgcn_raw_buffer_load_b128(rws, off + 16 * i, 0, AUX_SC1);
      if (__all(tags_ok(r[0], ta, tb) && tags_ok(r[1], ta, tb) && tags_ok(r[2], ta, tb) && tags_ok(r[3], ta, tb))) break;
      __builtin_amdgcn_s_sleep(2);
    }
#pragma unroll
    for (int i = 0; i < 4; ++i) {
      const float s0 = sum32(__uint_as_float(r[i][0])), s1 = sum32(__uint_as_float(r[i][2]));
      if ((lane & 31) == 0) { fin[wave * 16 + half * 8 + 2 * i] = s0; fin[wave * 16 + half * 8 + 2 * i + 1] = s1; }
    }
    __syncthreads();
    if (tid < 8) {
      const float a = (fin[2 * tid] + fin[16 + 2 * tid]) + (fin[32 + 2 * tid] + fin[48 + 2 * tid]);
      const float cc = (fin[2 * tid + 1] + fin[16 + 2 * tid + 1]) + (fin[32 + 2 * tid + 1] + fin[48 + 2 * tid + 1]);
      ((unsigned*)p.out)[(tbr * D + 16 * c) / 2 + tid] = f2bf(a) | (f2bf(cc) << 16);
    }
    if (bid == 0 && tid == 0) __hip_atomic_store((GAS unsigned long long*)(p.ws + WS_EPOCH), epoch + 1ull, __ATOMIC_RELAXED, __HIP_MEMORY_SCOPE_AGENT);
  }
}

extern "C" void kernel_launch(void* const* d_in, const int* in_sizes, int n_in, void* d_out, int out_size, void* d_ws, size_t ws_size, hipStream_t stream) {
  Params p;
  for (int b = 0; b < 2; ++b) {
    p.x[b] = (const uint16_t*)d_in[5 * b + 0];
    p.k[b] = (const uint16_t*)d_in[5 * b + 1];
    p.v[b] = (const uint16_t*)d_in[5 * b + 2];
    p.wq[b] = (const uint16_t*)d_in[5 * b + 3];
    p.wo[b] = (const uint16_t*)d_in[5 * b + 4];
  }
  p.out = (uint16_t*)d_out;
  p.ws = (unsigned char*)d_ws;
  (void)hipFuncSetAttribute((const void*)k_fused, hipFuncAttributeMaxDynamicSharedMemorySize, LDS_BYTES);
  k_fused<<<NWG, NTHR, LDS_BYTES, stream>>>(p);
}
```

```cpp
#include <hip/hip_runtime.h>
#include <hip/hip_bf16.h>
#include <stdint.h>

typedef unsigned u32x4 __attribute__((ext_vector_type(4)));
typedef unsigned u32x2 __attribute__((ext_vector_type(2)));
typedef unsigned u32x4a8 __attribute__((ext_vector_type(4), aligned(8)));
typedef float f32x4 __attribute__((ext_vector_type(4)));
typedef __bf16 bf16x2 __attribute__((ext_vector_type(2)));
#define GAS __attribute__((address_space(1)))

constexpr int D = 640, SK = 32768, DH = 192, HD = 32 * DH;
constexpr int NWG = 256, NWAVES = 4, NTHR = 256;
constexpr int WPG = 32;
constexpr unsigned HB = SK * DH * 2;
constexpr unsigned BUFB = 32u * HB;
constexpr unsigned UNITB = 128u * DH * 2u;
constexpr int NB = 32;
#define NDEEP 0
#define GMAP 1
#define SKEW 0
#define YPRE 0
#define KVAUX 2
constexpr int AUX_SC1 = 16;
constexpr unsigned SPIN_CAP = 1u << 20;

constexpr int WS_EPOCH = 0;
constexpr int WS_Q = 4096;
constexpr int WS_H1 = 65536;
constexpr int WS_H2 = WS_H1 + NWG * 16384;
constexpr int WS_END = WS_H2 + NWG * 5120;

constexpr int PREC = 196;
constexpr int L_REC = 0;
constexpr int L_Q = L_REC + 8 * NWAVES * PREC * 4;
constexpr int L_S = L_Q + 8 * 96 * 4;
constexpr int L_RED = L_S + NWAVES * NB * 32 * 4;
constexpr int L_QR = L_RED + NWAVES * D * 4;
constexpr int L_FIN = L_QR + 256 * 4;
constexpr int L_TR = L_FIN + 64 * 4;
constexpr int L_END = L_TR + 4 * 128 * 4;
constexpr int LDS_BYTES = 84 * 1024;
static_assert(L_END <= LDS_BYTES, "lds");

struct Params {
  const uint16_t* x[2];
  const uint16_t* k[2];
  const uint16_t* v[2];
  const uint16_t* wq[2];
  const uint16_t* wo[2];
  uint16_t* out;
  unsigned char* ws;
};

__device__ __forceinline__ float lo2f(unsigned u) { return __uint_as_float(u << 16); }
__device__ __forceinline__ float hi2f(unsigned u) { return __uint_as_float(u & 0xffff0000u); }
__device__ __forceinline__ unsigned f2bf(float f) {
  unsigned u = __float_as_uint(f);
  u += 0x7FFFu + ((u >> 16) & 1u);
  return u >> 16;
}
__device__ __forceinline__ float dot2(unsigned k, unsigned q, float acc) {
  return __builtin_amdgcn_fdot2_f32_bf16(__builtin_bit_cast(bf16x2, k), __builtin_bit_cast(bf16x2, q), acc, false);
}
template <int CTRL> __device__ __forceinline__ float dpp(float x) {
  return __int_as_float(__builtin_amdgcn_mov_dpp(__float_as_int(x), CTRL, 0xf, 0xf, true));
}
constexpr int XOR1 = 0xB1, XOR2 = 0x4E, XOR7 = 0x141, ROR8 = 0x128;
__device__ __forceinline__ float swap32_sum(float a, float b) {
  auto s = __builtin_amdgcn_permlane32_swap(__float_as_uint(a), __float_as_uint(b), false, false);
  return __uint_as_float(s[0]) + __uint_as_float(s[1]);
}
__device__ __forceinline__ float swap16_sum(float a, float b) {
  auto s = __builtin_amdgcn_permlane16_swap(__float_as_uint(a), __float_as_uint(b), false, false);
  return __uint_as_float(s[0]) + __uint_as_float(s[1]);
}
__device__ __forceinline__ float swap32_max(float a) {
  auto s = __builtin_amdgcn_permlane32_swap(__float_as_uint(a), __float_as_uint(a), false, false);
  return fmaxf(__uint_as_float(s[0]), __uint_as_float(s[1]));
}
__device__ __forceinline__ float swap16_max(float a) {
  auto s = __builtin_amdgcn_permlane16_swap(__float_as_uint(a), __float_as_uint(a), false, false);
  return fmaxf(__uint_as_float(s[0]), __uint_as_float(s[1]));
}
__device__ __forceinline__ float sum32(float x) {
  x += dpp<XOR1>(x); x += dpp<XOR2>(x); x += dpp<XOR7>(x); x += dpp<ROR8>(x);
  return swap16_sum(x, x);
}
__device__ __forceinline__ float max32(float x) {
  x = fmaxf(x, dpp<XOR1>(x)); x = fmaxf(x, dpp<XOR2>(x)); x = fmaxf(x, dpp<XOR7>(x)); x = fmaxf(x, dpp<ROR8>(x));
  return swap16_max(x);
}
__device__ __forceinline__ bool tags_ok(const u32x4& r, unsigned ta, unsigned tb) { return r[1] == ta && r[3] == tb; }
__device__ __forceinline__ u32x4 mkrec(unsigned a, unsigned b, unsigned ta, unsigned tb) { return u32x4{a, ta, b, tb}; }

struct Slot { u32x4 d[3]; };

__global__ void __launch_bounds__(NTHR, 1) k_fused(Params p) {
  extern __shared__ __attribute__((aligned(16))) unsigned char smem[];
  float* const sm = (float*)(smem + L_REC);
  unsigned* const sq = (unsigned*)(smem + L_Q);
  float* const sred = (float*)(smem + L_RED);
  float* const sqr = (float*)(smem + L_QR);
  float* const fin = (float*)(smem + L_FIN);

  const int bid = blockIdx.x, g = GMAP ? bid % 8 : bid / WPG, j = GMAP ? bid / 8 : bid % WPG;
  const int tid = threadIdx.x, lane = tid & 63, wave = __builtin_amdgcn_readfirstlane(tid >> 6);
  const int c8 = lane & 7, tt = lane >> 3;
#define STAMP(i)
#define TRACE(idx)
  STAMP(0);

  const __amdgpu_buffer_rsrc_t rws = __builtin_amdgcn_make_buffer_rsrc((void*)p.ws, 0, WS_END, 0x00020000);
  int eoff;
  asm volatile("v_mov_b32 %0, %1" : "=v"(eoff) : "s"(WS_EPOCH));
  const u32x2 ep = __builtin_amdgcn_raw_buffer_load_b64(rws, eoff, 0, AUX_SC1);

  u32x4 w[20];
  u32x4 xa, xb; u32x2 xc;
  const bool qprod = j < 24;
  if (qprod) {
    const int hs = j / 3, part = j % 3, qb = hs >> 2, qh = 8 * (hs & 3) + g;
    const int rr = tid >> 3;
    const uint16_t* x = p.x[qb] + rr * 20;
    xa = *(const GAS u32x4a8*)(x);
    xb = *(const GAS u32x4a8*)(x + 8);
    xc = *(const GAS u32x2*)(x + 16);
    __builtin_amdgcn_sched_barrier(0);
    const uint16_t* wq = p.wq[qb] + (size_t)(rr * 20) * HD + qh * DH + part * 64 + c8 * 8;
#pragma unroll
    for (int i = 0; i < 20; ++i) w[i] = __builtin_nontemporal_load((const GAS u32x4*)(wq + (size_t)i * HD));
  }
  __builtin_amdgcn_sched_barrier(0);
  const int v0 = tt * 384 + c8 * 16, v1 = v0 + 128, v2 = v0 + 256;
  constexpr unsigned HS1 = 3072, HS2 = 6144, HS3 = 9216;
  const unsigned wbase = g * HB + j * UNITB + wave * 12288;
  auto uoff = [&](unsigned n) -> unsigned { return (n >> 3) * (8u * HB) + ((n + SKEW * g) & 7u) * (32u * UNITB) + wbase; };
  auto mkr = [&](const uint16_t* b, unsigned n) { return __builtin_amdgcn_make_buffer_rsrc((void*)b, 0, n, 0x00020000); };
#define LDS_(S, rs, so) { S.d[0] = __builtin_amdgcn_raw_buffer_load_b128(rs, v0, (so), KVAUX); S.d[1] = __builtin_amdgcn_raw_buffer_load_b128(rs, v1, (so), KVAUX); S.d[2] = __builtin_amdgcn_raw_buffer_load_b128(rs, v2, (so), KVAUX); }
  Slot S0, S1, S2, S3, X0, X1, X2, X3;
  const __amdgpu_buffer_rsrc_t rk0 = mkr(p.k[0], BUFB);
  {
    const unsigned o0 = uoff(0);
    LDS_(X0, rk0, o0); LDS_(X1, rk0, o0 + HS1); LDS_(X2, rk0, o0 + HS2); LDS_(X3, rk0, o0 + HS3);
    LDS_(S0, rk0, uoff(1)); LDS_(S1, rk0, uoff(1) + HS1);
  }
  __builtin_amdgcn_sched_barrier(0);

  if (qprod) {
    float a[8] = {0, 0, 0, 0, 0, 0, 0, 0};
    const unsigned xs[10] = {xa[0], xa[1], xa[2], xa[3], xb[0], xb[1], xb[2], xb[3], xc[0], xc[1]};
#pragma unroll
    for (int i = 0; i < 20; ++i) {
      const unsigned xlo = (i & 1) ? xs[i >> 1] >> 16 : xs[i >> 1] & 0xffffu, xhi = xlo << 16;
#pragma unroll
      for (int e = 0; e < 4; ++e) { a[2 * e] = dot2(w[i][e], xlo, a[2 * e]); a[2 * e + 1] = dot2(w[i][e], xhi, a[2 * e + 1]); }
    }
    float r4[4], r2[2];
#pragma unroll
    for (int e = 0; e < 4; ++e) r4[e] = swap32_sum(a[e], a[e + 4]);
#pragma unroll
    for (int e = 0; e < 2; ++e) r2[e] = swap16_sum(r4[e], r4[e + 2]);
    const float u0 = r2[0] + dpp<ROR8>(r2[0]), u1 = r2[1] + dpp<ROR8>(r2[1]);
    const int e = (lane >= 32 ? 4 : 0) + ((lane & 16) ? 2 : 0) + ((lane & 8) ? 1 : 0);
    sqr[wave * 64 + c8 * 8 + e] = (lane & 8) ? u1 : u0;
  }
  const unsigned long long epoch = ((unsigned long long)ep[1] << 32) | ep[0];
  unsigned long long z = epoch + 0x9E3779B97F4A7C15ull + 1ull;
  z = (z ^ (z >> 30)) * 0xBF58476D1CE4E5B9ull; z = (z ^ (z >> 27)) * 0x94D049BB133111EBull; z ^= z >> 31;
  const unsigned ta = (unsigned)z, tb = (unsigned)(z >> 32);
  __syncthreads();
  if (qprod && wave == 0) {
    const float s = (sqr[lane] + sqr[64 + lane]) + (sqr[128 + lane] + sqr[192 + lane]);
    const float b = __uint_as_float(f2bf(s));
    const unsigned b0 = __float_as_uint(dpp<0x00>(b)), b1 = __float_as_uint(dpp<0x55>(b)), b2 = __float_as_uint(dpp<0xAA>(b)), b3 = __float_as_uint(dpp<0xFF>(b));
    if ((lane & 3) == 0)
      __builtin_amdgcn_raw_buffer_store_b128(mkrec(b0 | (b1 << 16), b2 | (b3 << 16), ta, tb), rws, WS_Q + (g * 384 + j * 16 + (lane >> 2)) * 16, 0, AUX_SC1);
  }
  STAMP(1);
  {
    const int r0 = wave * 96 + lane, r1 = wave * 96 + 64 + (lane & 31);
    u32x4 a, b;
    for (unsigned spin = 0; spin < SPIN_CAP; ++spin) {
      asm volatile("" ::: "memory");
      a = __builtin_amdgcn_raw_buffer_load_b128(rws, WS_Q + (g * 384 + r0) * 16, 0, AUX_SC1);
      b = __builtin_amdgcn_raw_buffer_load_b128(rws, WS_Q + (g * 384 + r1) * 16, 0, AUX_SC1);
      if (__all(tags_ok(a, ta, tb) && tags_ok(b, ta, tb))) break;
      __builtin_amdgcn_s_sleep(2);
    }
    *(u32x2*)&sq[2 * r0] = u32x2{a[0], a[2]};
    *(u32x2*)&sq[2 * r1] = u32x2{b[0], b[2]};
  }
  __syncthreads();
  STAMP(2);

  constexpr float C2 = 0.07216878364870322f * 1.4426950408889634f;
  float* const sp = (float*)(smem + L_S) + wave * (NB * 32);
  u32x4 qf[3];
  float mx, lsum;
  float acc[24];
  auto kstep = [&](const Slot& S, float* dst) {
    float s0 = 0.f, s1 = 0.f, s2 = 0.f;
#pragma unroll
    for (int e = 0; e < 4; ++e) { s0 = dot2(S.d[0][e], qf[0][e], s0); s1 = dot2(S.d[1][e], qf[1][e], s1); s2 = dot2(S.d[2][e], qf[2][e], s2); }
    float s = (s0 + s1) + s2;
    s += dpp<XOR1>(s); s += dpp<XOR2>(s); s += dpp<XOR7>(s);
    mx = fmaxf(mx, s);
    if (c8 == 0) *dst = s;
  };
  auto vstep = [&](const Slot& S, float pr) {
#pragma unroll
    for (int i = 0; i < 3; ++i)
#pragma unroll
      for (int e = 0; e < 4; ++e) {
        acc[i * 8 + 2 * e] = fmaf(pr, lo2f(S.d[i][e]), acc[i * 8 + 2 * e]);
        acc[i * 8 + 2 * e + 1] = fmaf(pr, hi2f(S.d[i][e]), acc[i * 8 + 2 * e + 1]);
      }
  };
#define SB() __builtin_amdgcn_sched_barrier(0)
#define PIN_ACC() asm volatile("" : "+v"(acc[0]), "+v"(acc[1]), "+v"(acc[2]), "+v"(acc[3]), "+v"(acc[4]), "+v"(acc[5]), "+v"(acc[6]), "+v"(acc[7]), \
    "+v"(acc[8]), "+v"(acc[9]), "+v"(acc[10]), "+v"(acc[11]), "+v"(acc[12]), "+v"(acc[13]), "+v"(acc[14]), "+v"(acc[15]), \
    "+v"(acc[16]), "+v"(acc[17]), "+v"(acc[18]), "+v"(acc[19]), "+v"(acc[20]), "+v"(acc[21]), "+v"(acc[22]), "+v"(acc[23]) :: "memory")
#define KHALF(CUR, NXT, RS, OFF, DST) { kstep(CUR, DST); SB(); LDS_(NXT, RS, OFF); SB(); }
#define VHALF(CUR, NXT, RS, OFF, PR) { vstep(CUR, PR); PIN_ACC(); SB(); LDS_(NXT, RS, OFF); SB(); }
  const int ths = j >> 2, tbr = ths >> 2, trow = (8 * (ths & 3) + g) * DH + (j & 3) * 48 + wave * 12;
  u32x4 wa[12]; unsigned wb[12];
  auto load_q = [&](int hs) {
#pragma unroll
    for (int i = 0; i < 3; ++i) qf[i] = *(const u32x4*)&sq[hs * 96 + 32 * i + 4 * c8];
    mx = -INFINITY;
  };
  auto khead_end = [&](int hs) {
    float M = fmaxf(mx, dpp<ROR8>(mx)); M = swap16_max(M); M = swap32_max(M);
    if (lane == 0) sm[(hs * NWAVES + wave) * PREC] = M * C2;
  };
  auto vflush = [&](int hs) {
    float r12[12], r6[6], r3[3];
#pragma unroll
    for (int a = 0; a < 12; ++a) r12[a] = swap32_sum(acc[a], acc[a + 12]);
#pragma unroll
    for (int a = 0; a < 6; ++a) r6[a] = swap16_sum(r12[a], r12[a + 6]);
#pragma unroll
    for (int a = 0; a < 3; ++a) {
      const float u = r6[a] + dpp<ROR8>(r6[a]), v = r6[a + 3] + dpp<ROR8>(r6[a + 3]);
      r3[a] = (lane & 8) ? v : u;
    }
    float l = lsum + dpp<ROR8>(lsum);
    l = swap16_sum(l, l); l = swap32_sum(l, l);
    float* rec = sm + (hs * NWAVES + wave) * PREC;
    const int abase = (lane >= 32 ? 12 : 0) + ((lane & 16) ? 6 : 0) + ((lane & 8) ? 3 : 0);
#pragma unroll
    for (int a = 0; a < 3; ++a) { const int ai = abase + a; rec[4 + 64 * (ai >> 3) + 8 * c8 + (ai & 7)] = r3[a]; }
    if (lane == 0) rec[1] = l;
#pragma unroll
    for (int a = 0; a < 24; ++a) acc[a] = 0.f;
    lsum = 0.f;
  };
  auto probs = [&](int n, float Ms, float& p0, float& p1, float& p2, float& p3) {
    const float* src = sp + n * 32 + tt;
    p0 = __builtin_amdgcn_exp2f(fmaf(src[0], C2, -Ms)); p1 = __builtin_amdgcn_exp2f(fmaf(src[8], C2, -Ms));
    p2 = __builtin_amdgcn_exp2f(fmaf(src[16], C2, -Ms)); p3 = __builtin_amdgcn_exp2f(fmaf(src[24], C2, -Ms));
    lsum += (p0 + p1) + (p2 + p3);
  };
#pragma unroll
  for (int a = 0; a < 24; ++a) acc[a] = 0.f;
  lsum = 0.f;

  TRACE(0);
  load_q(0);
  kstep(X0, sp + tt); kstep(X1, sp + 8 + tt); kstep(X2, sp + 16 + tt); kstep(X3, sp + 24 + tt);
  SB();
  TRACE(127);
  float Ms = 0.f;
#pragma unroll 1
  for (int br = 0; br < 2; ++br) {
    const uint16_t* kb = br ? p.k[1] : p.k[0];
    const uint16_t* vb = br ? p.v[1] : p.v[0];
    {
      const __amdgpu_buffer_rsrc_t rc = mkr(kb, BUFB), rn = mkr(vb, BUFB);
      const int n0 = __builtin_amdgcn_readfirstlane(br ? 0 : 1 + YPRE);
#pragma unroll 1
      for (int n = n0; n < NB; ++n) {
        TRACE(br * 64 + n);
        if ((n & 7) == 0) { if (n) khead_end(br * 4 + (n >> 3) - 1); load_q(br * 4 + (n >> 3)); }
        float* dst = sp + n * 32 + tt;
        const unsigned o = uoff(n);
        const bool last = n == NB - 1;
        const __amdgpu_buffer_rsrc_t rl = last ? rn : rc;
        const unsigned on = uoff(last ? 0 : n + 1);
        KHALF(S0, S2, rc, o + HS2, dst)
        KHALF(S1, S3, rc, o + HS3, dst + 8)
        KHALF(S2, S0, rl, on, dst + 16)
        KHALF(S3, S1, rl, on + HS1, dst + 24)
      }
      khead_end(br * 4 + 3);
    }
    if (br == 0) { STAMP(3); } else { STAMP(5); }
    {
      const __amdgpu_buffer_rsrc_t rc = mkr(vb, BUFB), rn = mkr(p.k[1], BUFB);
      const int nend = br ? NB - 1 - NDEEP : NB;
#pragma unroll 1
      for (int n = 0; n < nend; ++n) {
        TRACE(br * 64 + 32 + n);
        if ((n & 7) == 0) { if (n) vflush(br * 4 + (n >> 3) - 1); Ms = sm[((br * 4 + (n >> 3)) * NWAVES + wave) * PREC]; }
        float p0, p1, p2, p3;
        probs(n, Ms, p0, p1, p2, p3);
        const bool last = n == NB - 1;
        const __amdgpu_buffer_rsrc_t rl = last ? rn : rc;
        const unsigned o = uoff(n), on = uoff(last ? 0 : n + 1);
        VHALF(S0, S2, rc, o + HS2, p0)
        VHALF(S1, S3, rc, o + HS3, p1)
        VHALF(S2, S0, rl, on, p2)
        VHALF(S3, S1, rl, on + HS1, p3)
      }
      if (br == 0) { vflush(3); STAMP(4); }
    }
  }
  {
    const __amdgpu_buffer_rsrc_t rc = mkr(p.v[1], BUFB);
#pragma unroll 1
    for (int n = NB - 1 - NDEEP; n < NB - 1; ++n) {
      if ((n & 7) == 0) { vflush(4 + (n >> 3) - 1); Ms = sm[((4 + (n >> 3)) * NWAVES + wave) * PREC]; }
      float p0, p1, p2, p3;
      probs(n, Ms, p0, p1, p2, p3);
      const unsigned o = uoff(n), on = uoff(n + 1);
      LDS_(S2, rc, o + HS2); LDS_(S3, rc, o + HS3); SB();
      VHALF(S0, S0, rc, on, p0)
      VHALF(S1, S1, rc, on + HS1, p1)
      vstep(S2, p2); PIN_ACC(); SB();
      vstep(S3, p3); PIN_ACC(); SB();
    }
    if (((NB - 1) & 7) == 0) { vflush(4 + ((NB - 1) >> 3) - 1); Ms = sm[((4 + ((NB - 1) >> 3)) * NWAVES + wave) * PREC]; }
    float p0, p1, p2, p3;
    probs(NB - 1, Ms, p0, p1, p2, p3);
    const unsigned o = uoff(NB - 1);
    LDS_(S2, rc, o + HS2); LDS_(S3, rc, o + HS3); SB();
    const __amdgpu_buffer_rsrc_t rwo = __builtin_amdgcn_make_buffer_rsrc((void*)(p.wo[tbr] + (size_t)trow * D), 0, 12 * D * 2, 0x00020000);
#pragma unroll
    for (int r = 0; r < 12; ++r) {
      wa[r] = __builtin_amdgcn_raw_buffer_load_b128(rwo, lane * 20, r * D * 2, KVAUX);
      wb[r] = __builtin_amdgcn_raw_buffer_load_b32(rwo, lane * 20 + 16, r * D * 2, KVAUX);
    }
    SB(); vstep(S0, p0); PIN_ACC(); SB();
    vstep(S1, p1); PIN_ACC(); SB();
    vstep(S2, p2); PIN_ACC(); SB();
    vstep(S3, p3); PIN_ACC(); SB();
    vflush(7);
  }
  STAMP(6);
  __syncthreads();
  STAMP(7);

#pragma unroll
  for (int i = 0; i < 4; ++i) {
    const int R = tid + 256 * i, hd = R >> 7, sl = (R >> 3) & 15, k = R & 7;
    const float* rec = sm + hd * NWAVES * PREC;
    const float m0 = rec[0], m1 = rec[PREC], m2 = rec[2 * PREC], m3 = rec[3 * PREC];
    const float M = fmaxf(fmaxf(m0, m1), fmaxf(m2, m3));
    const float e0 = __builtin_amdgcn_exp2f(m0 - M), e1 = __builtin_amdgcn_exp2f(m1 - M), e2 = __builtin_amdgcn_exp2f(m2 - M), e3 = __builtin_amdgcn_exp2f(m3 - M);
    const bool hdr = (k == 0 || k == 7);
    const int ia = hdr ? 1 : 4 + 12 * sl + 2 * (k - 1), ib = hdr ? 1 : ia + 1;
    const float va = (e0 * rec[ia] + e1 * rec[PREC + ia]) + (e2 * rec[2 * PREC + ia] + e3 * rec[3 * PREC + ia]);
    const float vb = (e0 * rec[ib] + e1 * rec[PREC + ib]) + (e2 * rec[2 * PREC + ib] + e3 * rec[3 * PREC + ib]);
    __builtin_amdgcn_raw_buffer_store_b128(mkrec(__float_as_uint(hdr ? M : va), __float_as_uint(vb), ta, tb), rws, WS_H1 + (g * WPG + j) * 16384 + R * 16, 0, AUX_SC1);
  }
  unsigned o6[3][2];
  {
    const int jj = lane & 31, half = lane >> 5;
    const int off = WS_H1 + (g * WPG + jj) * 16384 + ths * 2048 + ((j & 3) * 4 + wave) * 128 + half * 64;
    u32x4 r[4];
    for (unsigned spin = 0; spin < SPIN_CAP; ++spin) {
      asm volatile("" ::: "memory");
#pragma unroll
      for (int i = 0; i < 4; ++i) r[i] = __builtin_amdgcn_raw_buffer_load_b128(rws, off + 16 * i, 0, AUX_SC1);
      if (__all(tags_ok(r[0], ta, tb) && tags_ok(r[1], ta, tb) && tags_ok(r[2], ta, tb) && tags_ok(r[3], ta, tb))) break;
      __builtin_amdgcn_s_sleep(2);
    }
    const float mj = __uint_as_float(half ? r[3][0] : r[0][0]), lj = __uint_as_float(half ? r[3][2] : r[0][2]);
    const float M = max32(mj);
    const float wj = __builtin_amdgcn_exp2f(mj - M);
    const float inv = 1.f / sum32(wj * lj);
#pragma unroll
    for (int i = 0; i < 3; ++i) {
      const u32x4 q = half ? r[i] : r[i + 1];
      o6[i][0] = f2bf(sum32(wj * __uint_as_float(q[0])) * inv);
      o6[i][1] = f2bf(sum32(wj * __uint_as_float(q[2])) * inv);
    }
  }
  STAMP(8);
  {
    float y[10] = {0, 0, 0, 0, 0, 0, 0, 0, 0, 0};
#pragma unroll
    for (int r = 0; r < 12; ++r) {
      const unsigned olo = (unsigned)__builtin_amdgcn_readlane((int)o6[(r % 6) >> 1][r & 1], r < 6 ? 0 : 32), ohi = olo << 16;
#pragma unroll
      for (int e = 0; e < 4; ++e) { y[2 * e] = dot2(wa[r][e], olo, y[2 * e]); y[2 * e + 1] = dot2(wa[r][e], ohi, y[2 * e + 1]); }
      y[8] = dot2(wb[r], olo, y[8]); y[9] = dot2(wb[r], ohi, y[9]);
    }
#pragma unroll
    for (int e = 0; e < 5; ++e) *(float2*)&sred[wave * D + lane * 10 + 2 * e] = make_float2(y[2 * e], y[2 * e + 1]);
  }
  __syncthreads();
#pragma unroll
  for (int i = 0; i < 2; ++i) {
    const int n = tid + 256 * i;
    if (n < 320) {
      const float a = (sred[2 * n] + sred[D + 2 * n]) + (sred[2 * D + 2 * n] + sred[3 * D + 2 * n]);
      const float b = (sred[2 * n + 1] + sred[D + 2 * n + 1]) + (sred[2 * D + 2 * n + 1] + sred[3 * D + 2 * n + 1]);
      __builtin_amdgcn_raw_buffer_store_b128(mkrec(__float_as_uint(a), __float_as_uint(b), ta, tb), rws, WS_H2 + (tbr * 128 + g * 16 + (j & 15)) * 5120 + n * 16, 0, AUX_SC1);
    }
  }
  STAMP(9);
  if ((j & 15) < 5) {
    const int c = g * 5 + (j & 15);
    const int pp = wave * 32 + (lane & 31), half = lane >> 5;
    const int off = WS_H2 + (tbr * 128 + pp) * 5120 + (8 * c + 4 * half) * 16;
    u32x4 r[4];
    for (unsigned spin = 0; spin < SPIN_CAP; ++spin) {
      asm volatile("" ::: "memory");
#pragma unroll
      for (int i = 0; i < 4; ++i) r[i] = __builtin_amdgcn_raw_buffer_load_b128(rws, off + 16 * i, 0, AUX_SC1);
      if (__all(tags_ok(r[0], ta, tb) && tags_ok(r[1], ta, tb) && tags_ok(r[2], ta, tb) && tags_ok(r[3], ta, tb))) break;
      __builtin_amdgcn_s_sleep(2);
    }
#pragma unroll
    for (int i = 0; i < 4; ++i) {
      const float s0 = sum32(__uint_as_float(r[i][0])), s1 = sum32(__uint_as_float(r[i][2]));
      if ((lane & 31) == 0) { fin[wave * 16 + half * 8 + 2 * i] = s0; fin[wave * 16 + half * 8 + 2 * i + 1] = s1; }
    }
    __syncthreads();
    if (tid < 8) {
      const float a = (fin[2 * tid] + fin[16 + 2 * tid]) + (fin[32 + 2 * tid] + fin[48 + 2 * tid]);
      const float cc = (fin[2 * tid + 1] + fin[16 + 2 * tid + 1]) + (fin[32 + 2 * tid + 1] + fin[48 + 2 * tid + 1]);
      ((unsigned*)p.out)[(tbr * D + 16 * c) / 2 + tid] = f2bf(a) | (f2bf(cc) << 16);
    }
    if (bid == 0 && tid == 0) __hip_atomic_store((GAS unsigned long long*)(p.ws + WS_EPOCH), epoch + 1ull, __ATOMIC_RELAXED, __HIP_MEMORY_SCOPE_AGENT);
  }
}

extern "C" void kernel_launch(void* const* d_in, const int* in_sizes, int n_in, void* d_out, int out_size, void* d_ws, size_t ws_size, hipStream_t stream) {
  Params p;
  for (int b = 0; b < 2; ++b) {
    p.x[b] = (const uint16_t*)d_in[5 * b + 0];
    p.k[b] = (const uint16_t*)d_in[5 * b + 1];
    p.v[b] = (const uint16_t*)d_in[5 * b + 2];
    p.wq[b] = (const uint16_t*)d_in[5 * b + 3];
    p.wo[b] = (const uint16_t*)d_in[5 * b + 4];
  }
  p.out = (uint16_t*)d_out;
  p.ws = (unsigned char*)d_ws;
  (void)hipFuncSetAttribute((const void*)k_fused, hipFuncAttributeMaxDynamicSharedMemorySize, LDS_BYTES);
  k_fused<<<NWG, NTHR, LDS_BYTES, stream>>>(p);
}
```

```cpp
#include <hip/hip_runtime.h>
#include <hip/hip_bf16.h>
#include <stdint.h>

typedef unsigned u32x4 __attribute__((ext_vector_type(4)));
typedef unsigned u32x2 __attribute__((ext_vector_type(2)));
typedef unsigned u32x4a8 __attribute__((ext_vector_type(4), aligned(8)));
typedef float f32x4 __attribute__((ext_vector_type(4)));
typedef __bf16 bf16x2 __attribute__((ext_vector_type(2)));
#define GAS __attribute__((address_space(1)))
#define LAS __attribute__((address_space(3)))
#define RLX_WG __ATOMIC_RELAXED, __HIP_MEMORY_SCOPE_WORKGROUP

constexpr int D = 640, SK = 32768, DH = 192, HD = 32 * DH;
constexpr int NWG = 256, NWAVES = 4, NTHR = 320;
constexpr int WPG = 32;
constexpr unsigned HB = SK * DH * 2;
constexpr unsigned BUFB = 32u * HB;
constexpr unsigned UNITB = 128u * DH * 2u;
constexpr int NB = 32;
#define KSL 3
#define VSL 3
#define PPER 4
#define SWSL 2
#define EARLYPUB 0
#define PSTAUX 16
#define PSTART 16
#define PSLOW 0
#define SGAIN 45
#define PGAIN 45
#define PDEAD 10
#define PACE 1
#define GMAP 1
#define SKEW 0
#define YPRE 0
#define KVAUX 2
constexpr int AUX_SC1 = 16;
constexpr unsigned SPIN_CAP = 1u << 20;

constexpr int WS_EPOCH = 0;
constexpr int WS_PACE = 6 << 20;
constexpr int WS_Q = 4096;
constexpr int WS_H1 = 65536;
constexpr int WS_H2 = WS_H1 + NWG * 16384;
constexpr int WS_END = WS_PACE + 64 * 8 * 32 * 8;
static_assert(WS_H2 + NWG * 5120 <= WS_PACE, "map");

constexpr int PREC = 196;
constexpr int L_REC = 0;
constexpr int L_Q = L_REC + 8 * NWAVES * PREC * 4;
constexpr int L_S = L_Q + 8 * 96 * 4;
constexpr int L_RED = L_S + NWAVES * NB * 32 * 4;
constexpr int L_QR = L_RED + NWAVES * D * 4;
constexpr int L_FIN = L_QR + 256 * 4;
constexpr int L_PACE = L_FIN + 64 * 4;
constexpr int L_TR = L_PACE + 16 + 32;
constexpr int L_END = L_TR + 4 * 128 * 4;
constexpr int LDS_BYTES = 84 * 1024;
static_assert(L_END <= LDS_BYTES, "lds");

struct Params {
  const uint16_t* x[2];
  const uint16_t* k[2];
  const uint16_t* v[2];
  const uint16_t* wq[2];
  const uint16_t* wo[2];
  uint16_t* out;
  unsigned char* ws;
};

__device__ __forceinline__ float lo2f(unsigned u) { return __uint_as_float(u << 16); }
__device__ __forceinline__ float hi2f(unsigned u) { return __uint_as_float(u & 0xffff0000u); }
__device__ __forceinline__ unsigned f2bf(float f) {
  unsigned u = __float_as_uint(f);
  u += 0x7FFFu + ((u >> 16) & 1u);
  return u >> 16;
}
__device__ __forceinline__ float dot2(unsigned k, unsigned q, float acc) {
  return __builtin_amdgcn_fdot2_f32_bf16(__builtin_bit_cast(bf16x2, k), __builtin_bit_cast(bf16x2, q), acc, false);
}
template <int CTRL> __device__ __forceinline__ float dpp(float x) {
  return __int_as_float(__builtin_amdgcn_mov_dpp(__float_as_int(x), CTRL, 0xf, 0xf, true));
}
constexpr int XOR1 = 0xB1, XOR2 = 0x4E, XOR7 = 0x141, ROR8 = 0x128;
__device__ __forceinline__ float swap32_sum(float a, float b) {
  auto s = __builtin_amdgcn_permlane32_swap(__float_as_uint(a), __float_as_uint(b), false, false);
  return __uint_as_float(s[0]) + __uint_as_float(s[1]);
}
__device__ __forceinline__ float swap16_sum(float a, float b) {
  auto s = __builtin_amdgcn_permlane16_swap(__float_as_uint(a), __float_as_uint(b), false, false);
  return __uint_as_float(s[0]) + __uint_as_float(s[1]);
}
__device__ __forceinline__ float swap32_max(float a) {
  auto s = __builtin_amdgcn_permlane32_swap(__float_as_uint(a), __float_as_uint(a), false, false);
  return fmaxf(__uint_as_float(s[0]), __uint_as_float(s[1]));
}
__device__ __forceinline__ float swap16_max(float a) {
  auto s = __builtin_amdgcn_permlane16_swap(__float_as_uint(a), __float_as_uint(a), false, false);
  return fmaxf(__uint_as_float(s[0]), __uint_as_float(s[1]));
}
__device__ __forceinline__ float sum32(float x) {
  x += dpp<XOR1>(x); x += dpp<XOR2>(x); x += dpp<XOR7>(x); x += dpp<ROR8>(x);
  return swap16_sum(x, x);
}
__device__ __forceinline__ float max32(float x) {
  x = fmaxf(x, dpp<XOR1>(x)); x = fmaxf(x, dpp<XOR2>(x)); x = fmaxf(x, dpp<XOR7>(x)); x = fmaxf(x, dpp<ROR8>(x));
  return swap16_max(x);
}
__device__ __forceinline__ bool tags_ok(const u32x4& r, unsigned ta, unsigned tb) { return r[1] == ta && r[3] == tb; }
__device__ __forceinline__ u32x4 mkrec(unsigned a, unsigned b, unsigned ta, unsigned tb) { return u32x4{a, ta, b, tb}; }

__device__ __forceinline__ u32x4 merged_record(const float* rec, int R, unsigned ta, unsigned tb) {
  const int sl = R >> 3, k = R & 7;
  const float m0 = rec[0], m1 = rec[PREC], m2 = rec[2 * PREC], m3 = rec[3 * PREC];
  const float M = fmaxf(fmaxf(m0, m1), fmaxf(m2, m3));
  const float e0 = __builtin_amdgcn_exp2f(m0 - M), e1 = __builtin_amdgcn_exp2f(m1 - M), e2 = __builtin_amdgcn_exp2f(m2 - M), e3 = __builtin_amdgcn_exp2f(m3 - M);
  const bool hdr = (k == 0 || k == 7);
  const int ia = hdr ? 1 : 4 + 12 * sl + 2 * (k - 1), ib = hdr ? 1 : ia + 1;
  const float va = (e0 * rec[ia] + e1 * rec[PREC + ia]) + (e2 * rec[2 * PREC + ia] + e3 * rec[3 * PREC + ia]);
  const float vb = (e0 * rec[ib] + e1 * rec[PREC + ib]) + (e2 * rec[2 * PREC + ib] + e3 * rec[3 * PREC + ib]);
  return mkrec(__float_as_uint(hdr ? M : va), __float_as_uint(vb), ta, tb);
}

__device__ __forceinline__ void sweep4(u32x4 (&r)[4], const __amdgpu_buffer_rsrc_t& rs, int off, unsigned ta, unsigned tb) {
  for (unsigned spin = 0; spin < SPIN_CAP; ++spin) {
    asm volatile("" ::: "memory");
#pragma unroll
    for (int i = 0; i < 4; ++i) r[i] = __builtin_amdgcn_raw_buffer_load_b128(rs, off + 16 * i, 0, AUX_SC1);
    if (__all(tags_ok(r[0], ta, tb) && tags_ok(r[1], ta, tb) && tags_ok(r[2], ta, tb) && tags_ok(r[3], ta, tb))) break;
    if (SWSL) __builtin_amdgcn_s_sleep(SWSL);
  }
}

struct Slot { u32x4 d[3]; };

__global__ void __launch_bounds__(NTHR, 2) k_fused(Params p) {
  extern __shared__ __attribute__((aligned(16))) unsigned char smem[];
  float* const sm = (float*)(smem + L_REC);
  unsigned* const sq = (unsigned*)(smem + L_Q);
  float* const sred = (float*)(smem + L_RED);
  float* const sqr = (float*)(smem + L_QR);
  float* const fin = (float*)(smem + L_FIN);

  const int bid = blockIdx.x, g = GMAP ? bid % 8 : bid / WPG, j = GMAP ? bid / 8 : bid % WPG;
  const int tid = threadIdx.x, lane = tid & 63, wave = __builtin_amdgcn_readfirstlane(tid >> 6);
  const int c8 = lane & 7, tt = lane >> 3;
#define STAMP(i)
#define TRACE(idx)
  STAMP(0);
  const uint16_t *px0 = p.x[0], *px1 = p.x[1], *pk0 = p.k[0], *pk1 = p.k[1], *pv0 = p.v[0], *pv1 = p.v[1], *pq0 = p.wq[0], *pq1 = p.wq[1], *po0 = p.wo[0], *po1 = p.wo[1];
  uint16_t* pout = p.out;
  unsigned char* pws = p.ws;
  asm volatile("" : "+s"(px0), "+s"(px1), "+s"(pk0), "+s"(pk1), "+s"(pv0), "+s"(pv1), "+s"(pq0), "+s"(pq1), "+s"(po0), "+s"(po1), "+s"(pout), "+s"(pws));

  LAS int* const pace = (LAS int*)(smem + L_PACE);
  if (wave == NWAVES) {
    if (lane < 12) pace[lane] = lane == 0 ? -1 : 0;
    const __amdgpu_buffer_rsrc_t rw = __builtin_amdgcn_make_buffer_rsrc((void*)pws, 0, WS_END, 0x00020000);
    const u32x2 e2 = __builtin_amdgcn_raw_buffer_load_b64(rw, WS_EPOCH, 0, AUX_SC1);
    unsigned long long z2 = (((unsigned long long)e2[1] << 32) | e2[0]) + 0x9E3779B97F4A7C15ull + 1ull;
    z2 = (z2 ^ (z2 >> 30)) * 0xBF58476D1CE4E5B9ull; z2 = (z2 ^ (z2 >> 27)) * 0x94D049BB133111EBull; z2 ^= z2 >> 31;
    const unsigned tag = (unsigned)z2, tagb = (unsigned)(z2 >> 32);
    asm volatile("s_waitcnt vmcnt(0) lgkmcnt(0)" ::: "memory");
    __builtin_amdgcn_s_barrier();
    __builtin_amdgcn_s_barrier();
    const int slot = (j & 7) * 32 + g * 4 + (j >> 3);
    unsigned tprev = 0; int kprev = 0, sprev = 0, nslot = 0;
    auto publish_slot = [&](int hs) {
      asm volatile("" ::: "memory");
#pragma unroll
      for (int i = 0; i < 2; ++i) {
        const int R = lane + 64 * i;
        __builtin_amdgcn_raw_buffer_store_b128(merged_record((const float*)(smem + L_REC) + hs * NWAVES * PREC, R, tag, tagb), rw, WS_H1 + (g * WPG + j) * 16384 + hs * 2048 + R * 16, 0, AUX_SC1);
      }
    };
    for (int c = PSTART; c < (127 + PPER - 1) / PPER; ++c) {
      for (unsigned spin = 0; spin < SPIN_CAP && __hip_atomic_load(pace, RLX_WG) < PPER * c; ++spin) __builtin_amdgcn_s_sleep(4);
      const unsigned now = (unsigned)__builtin_amdgcn_s_memrealtime();
      if (lane == 0) __builtin_amdgcn_raw_buffer_store_b64(u32x2{now, tag}, rw, WS_PACE + (c * 256 + slot) * 8, 0, PSTAUX);
      if (PACE && c > PSTART) {
        const u32x2 pr = __builtin_amdgcn_raw_buffer_load_b64(rw, WS_PACE + ((c - 1) * 256 + (j & 7) * 32 + (lane & 31)) * 8, 0, AUX_SC1);
        const float d = (float)(int)((pr[1] == tag ? pr[0] : now) - tprev);
        const float mean = sum32(d) * (1.f / 32.f);
        const int pred = (int)(-mean) - PGAIN * kprev + SGAIN * sprev;
        int k = (pred - PDEAD) / PGAIN, sl = PSLOW ? (-pred - PDEAD) / SGAIN : 0;
        k = __builtin_amdgcn_readfirstlane(k < 0 ? 0 : k > PPER - 1 ? PPER - 1 : k);
        sl = __builtin_amdgcn_readfirstlane(sl < 0 ? 0 : sl > PPER - 1 ? PPER - 1 : sl);
        if (lane == 0) { __hip_atomic_store(pace + 1, PPER * c + 2 + k, RLX_WG); __hip_atomic_store(pace + 2, PPER * c + 2 + sl, RLX_WG); }
        kprev = k; sprev = sl;
      }
      tprev = now;
      while (EARLYPUB && nslot < 7 && __hip_atomic_load(pace + 4 + nslot, RLX_WG) == NWAVES) { publish_slot(nslot); ++nslot; }
    }
    for (; EARLYPUB && nslot < 7; ++nslot) {
      for (unsigned spin = 0; spin < SPIN_CAP && __hip_atomic_load(pace + 4 + nslot, RLX_WG) != NWAVES; ++spin) __builtin_amdgcn_s_sleep(4);
      publish_slot(nslot);
    }
    return;
  }

  const __amdgpu_buffer_rsrc_t rws = __builtin_amdgcn_make_buffer_rsrc((void*)pws, 0, WS_END, 0x00020000);
  int eoff;
  asm volatile("v_mov_b32 %0, %1" : "=v"(eoff) : "s"(WS_EPOCH));
  const u32x2 ep = __builtin_amdgcn_raw_buffer_load_b64(rws, eoff, 0, AUX_SC1);

  u32x4 w[20];
  u32x4 xa, xb; u32x2 xc;
  const bool qprod = j < 24;
  if (qprod) {
    const int hs = j / 3, part = j % 3, qb = hs >> 2, qh = 8 * (hs & 3) + g;
    const int rr = tid >> 3;
    const __amdgpu_buffer_rsrc_t rx = __builtin_amdgcn_make_buffer_rsrc((void*)(qb ? px1 : px0), 0, D * 2, 0x00020000);
    xa = __builtin_amdgcn_raw_buffer_load_b128(rx, rr * 40, 0, 0);
    xb = __builtin_amdgcn_raw_buffer_load_b128(rx, rr * 40 + 16, 0, 0);
    xc = __builtin_amdgcn_raw_buffer_load_b64(rx, rr * 40 + 32, 0, 0);
    __builtin_amdgcn_sched_barrier(0);
    const __amdgpu_buffer_rsrc_t rq = __builtin_amdgcn_make_buffer_rsrc((void*)((qb ? pq1 : pq0) + qh * DH + part * 64), 0, D * HD * 2 - (qh * DH + part * 64) * 2, 0x00020000);
    const int qoff = rr * 20 * HD * 2 + c8 * 16;
#pragma unroll
    for (int i = 0; i < 20; ++i) w[i] = __builtin_amdgcn_raw_buffer_load_b128(rq, qoff, i * HD * 2, KVAUX);
  }
  __builtin_amdgcn_sched_barrier(0);
  const int v0 = tt * 384 + c8 * 16, v1 = v0 + 128, v2 = v0 + 256;
  constexpr unsigned HS1 = 3072, HS2 = 6144, HS3 = 9216;
  const unsigned wbase = g * HB + j * UNITB + wave * 12288;
  auto uoff = [&](unsigned n) -> unsigned { return (n >> 3) * (8u * HB) + ((n + SKEW * g) & 7u) * (32u * UNITB) + wbase; };
  auto mkr = [&](const uint16_t* b, unsigned n) { return __builtin_amdgcn_make_buffer_rsrc((void*)b, 0, n, 0x00020000); };
#define LDS_(S, rs, so) { S.d[0] = __builtin_amdgcn_raw_buffer_load_b128(rs, v0, (so), KVAUX); S.d[1] = __builtin_amdgcn_raw_buffer_load_b128(rs, v1, (so), KVAUX); S.d[2] = __builtin_amdgcn_raw_buffer_load_b128(rs, v2, (so), KVAUX); }
  Slot S0, S1, S2, S3, X0, X1, X2, X3;
  const __amdgpu_buffer_rsrc_t rk0 = mkr(pk0, BUFB);
  {
    const unsigned o0 = uoff(0);
    LDS_(X0, rk0, o0); LDS_(X1, rk0, o0 + HS1); LDS_(X2, rk0, o0 + HS2); LDS_(X3, rk0, o0 + HS3);
    LDS_(S0, rk0, uoff(1)); LDS_(S1, rk0, uoff(1) + HS1);
  }
  __builtin_amdgcn_sched_barrier(0);

  if (qprod) {
    float a[8] = {0, 0, 0, 0, 0, 0, 0, 0};
    const unsigned xs[10] = {xa[0], xa[1], xa[2], xa[3], xb[0], xb[1], xb[2], xb[3], xc[0], xc[1]};
#pragma unroll
    for (int i = 0; i < 20; ++i) {
      const unsigned xlo = (i & 1) ? xs[i >> 1] >> 16 : xs[i >> 1] & 0xffffu, xhi = xlo << 16;
#pragma unroll
      for (int e = 0; e < 4; ++e) { a[2 * e] = dot2(w[i][e], xlo, a[2 * e]); a[2 * e + 1] = dot2(w[i][e], xhi, a[2 * e + 1]); }
    }
    float r4[4], r2[2];
#pragma unroll
    for (int e = 0; e < 4; ++e) r4[e] = swap32_sum(a[e], a[e + 4]);
#pragma unroll
    for (int e = 0; e < 2; ++e) r2[e] = swap16_sum(r4[e], r4[e + 2]);
    const float u0 = r2[0] + dpp<ROR8>(r2[0]), u1 = r2[1] + dpp<ROR8>(r2[1]);
    const int e = (lane >= 32 ? 4 : 0) + ((lane & 16) ? 2 : 0) + ((lane & 8) ? 1 : 0);
    sqr[wave * 64 + c8 * 8 + e] = (lane & 8) ? u1 : u0;
  }
  const unsigned long long epoch = ((unsigned long long)ep[1] << 32) | ep[0];
  unsigned long long z = epoch + 0x9E3779B97F4A7C15ull + 1ull;
  z = (z ^ (z >> 30)) * 0xBF58476D1CE4E5B9ull; z = (z ^ (z >> 27)) * 0x94D049BB133111EBull; z ^= z >> 31;
  const unsigned ta = (unsigned)z, tb = (unsigned)(z >> 32);
  __syncthreads();
  if (qprod && wave == 0) {
    const float s = (sqr[lane] + sqr[64 + lane]) + (sqr[128 + lane] + sqr[192 + lane]);
    const float b = __uint_as_float(f2bf(s));
    const unsigned b0 = __float_as_uint(dpp<0x00>(b)), b1 = __float_as_uint(dpp<0x55>(b)), b2 = __float_as_uint(dpp<0xAA>(b)), b3 = __float_as_uint(dpp<0xFF>(b));
    if ((lane & 3) == 0)
      __builtin_amdgcn_raw_buffer_store_b128(mkrec(b0 | (b1 << 16), b2 | (b3 << 16), ta, tb), rws, WS_Q + (g * 384 + j * 16 + (lane >> 2)) * 16, 0, AUX_SC1);
  }
  STAMP(1);
  {
    const int r0 = wave * 96 + lane, r1 = wave * 96 + 64 + (lane & 31);
    u32x4 a, b;
    for (unsigned spin = 0; spin < SPIN_CAP; ++spin) {
      asm volatile("" ::: "memory");
      a = __builtin_amdgcn_raw_buffer_load_b128(rws, WS_Q + (g * 384 + r0) * 16, 0, AUX_SC1);
      b = __builtin_amdgcn_raw_buffer_load_b128(rws, WS_Q + (g * 384 + r1) * 16, 0, AUX_SC1);
      if (__all(tags_ok(a, ta, tb) && tags_ok(b, ta, tb))) break;
      __builtin_amdgcn_s_sleep(2);
    }
    *(u32x2*)&sq[2 * r0] = u32x2{a[0], a[2]};
    *(u32x2*)&sq[2 * r1] = u32x2{b[0], b[2]};
  }
  __syncthreads();
  STAMP(2);

  constexpr float C2 = 0.07216878364870322f * 1.4426950408889634f;
  float* const sp = (float*)(smem + L_S) + wave * (NB * 32);
  u32x4 qf[3];
  float mx, lsum;
  float acc[24];
  auto kstep = [&](const Slot& S, float* dst) {
    float s0 = 0.f, s1 = 0.f, s2 = 0.f;
#pragma unroll
    for (int e = 0; e < 4; ++e) { s0 = dot2(S.d[0][e], qf[0][e], s0); s1 = dot2(S.d[1][e], qf[1][e], s1); s2 = dot2(S.d[2][e], qf[2][e], s2); }
    float s = (s0 + s1) + s2;
    s += dpp<XOR1>(s); s += dpp<XOR2>(s); s += dpp<XOR7>(s);
    mx = fmaxf(mx, s);
    if (c8 == 0) *dst = s;
  };
  auto vstep = [&](const Slot& S, float pr) {
#pragma unroll
    for (int i = 0; i < 3; ++i)
#pragma unroll
      for (int e = 0; e < 4; ++e) {
        acc[i * 8 + 2 * e] = fmaf(pr, lo2f(S.d[i][e]), acc[i * 8 + 2 * e]);
        acc[i * 8 + 2 * e + 1] = fmaf(pr, hi2f(S.d[i][e]), acc[i * 8 + 2 * e + 1]);
      }
  };
#define SB() __builtin_amdgcn_sched_barrier(0)
#define PIN_ACC() asm volatile("" : "+v"(acc[0]), "+v"(acc[1]), "+v"(acc[2]), "+v"(acc[3]), "+v"(acc[4]), "+v"(acc[5]), "+v"(acc[6]), "+v"(acc[7]), \
    "+v"(acc[8]), "+v"(acc[9]), "+v"(acc[10]), "+v"(acc[11]), "+v"(acc[12]), "+v"(acc[13]), "+v"(acc[14]), "+v"(acc[15]), \
    "+v"(acc[16]), "+v"(acc[17]), "+v"(acc[18]), "+v"(acc[19]), "+v"(acc[20]), "+v"(acc[21]), "+v"(acc[22]), "+v"(acc[23]) :: "memory")
#define PACE_WAIT(SL) { if (!deep) { asm volatile("s_waitcnt vmcnt(3)" ::: "memory"); if (SL) __builtin_amdgcn_s_sleep(SL); if (PSLOW && slow) __builtin_amdgcn_s_sleep(PSLOW); } SB(); }
#define KHALF(CUR, NXT, RS, OFF, DST) { PACE_WAIT(KSL) LDS_(NXT, RS, OFF); SB(); kstep(CUR, DST); SB(); }
#define VHALF(CUR, NXT, RS, OFF, PR) { PACE_WAIT(VSL) LDS_(NXT, RS, OFF); SB(); vstep(CUR, PR); PIN_ACC(); SB(); }
  int pw_deep = 0, pw_slow = 0;
  auto pace_body = [&](int ng, bool& deep, bool& slow) {
    deep = ng < __builtin_amdgcn_readfirstlane(pw_deep);
    slow = ng < __builtin_amdgcn_readfirstlane(pw_slow);
    if (wave == 0 && lane == 0) __hip_atomic_store(pace, ng, RLX_WG);
    pw_deep = __hip_atomic_load(pace + 1, RLX_WG);
    pw_slow = __hip_atomic_load(pace + 2, RLX_WG);
  };
  const int ths = j >> 2, tbr = ths >> 2, trow = (8 * (ths & 3) + g) * DH + (j & 3) * 48 + wave * 12;
  u32x4 wa[12]; unsigned wb[12];
  auto load_q = [&](int hs) {
#pragma unroll
    for (int i = 0; i < 3; ++i) qf[i] = *(const u32x4*)&sq[hs * 96 + 32 * i + 4 * c8];
    mx = -INFINITY;
  };
  auto khead_end = [&](int hs) {
    float M = fmaxf(mx, dpp<ROR8>(mx)); M = swap16_max(M); M = swap32_max(M);
    if (lane == 0) sm[(hs * NWAVES + wave) * PREC] = M * C2;
  };
  auto vflush = [&](int hs) {
    float r12[12], r6[6], r3[3];
#pragma unroll
    for (int a = 0; a < 12; ++a) r12[a] = swap32_sum(acc[a], acc[a + 12]);
#pragma unroll
    for (int a = 0; a < 6; ++a) r6[a] = swap16_sum(r12[a], r12[a + 6]);
#pragma unroll
    for (int a = 0; a < 3; ++a) {
      const float u = r6[a] + dpp<ROR8>(r6[a]), v = r6[a + 3] + dpp<ROR8>(r6[a + 3]);
      r3[a] = (lane & 8) ? v : u;
    }
    float l = lsum + dpp<ROR8>(lsum);
    l = swap16_sum(l, l); l = swap32_sum(l, l);
    float* rec = sm + (hs * NWAVES + wave) * PREC;
    const int abase = (lane >= 32 ? 12 : 0) + ((lane & 16) ? 6 : 0) + ((lane & 8) ? 3 : 0);
#pragma unroll
    for (int a = 0; a < 3; ++a) { const int ai = abase + a; rec[4 + 64 * (ai >> 3) + 8 * c8 + (ai & 7)] = r3[a]; }
    if (lane == 0) rec[1] = l;
    asm volatile("" ::: "memory");
    if (lane == 0) __hip_atomic_fetch_add(pace + 4 + hs, 1, RLX_WG);
#pragma unroll
    for (int a = 0; a < 24; ++a) acc[a] = 0.f;
    lsum = 0.f;
  };
  auto probs = [&](int n, float Ms, float& p0, float& p1, float& p2, float& p3) {
    const float* src = sp + n * 32 + tt;
    p0 = __builtin_amdgcn_exp2f(fmaf(src[0], C2, -Ms)); p1 = __builtin_amdgcn_exp2f(fmaf(src[8], C2, -Ms));
    p2 = __builtin_amdgcn_exp2f(fmaf(src[16], C2, -Ms)); p3 = __builtin_amdgcn_exp2f(fmaf(src[24], C2, -Ms));
    lsum += (p0 + p1) + (p2 + p3);
  };
#pragma unroll
  for (int a = 0; a < 24; ++a) acc[a] = 0.f;
  lsum = 0.f;

  TRACE(0);
  load_q(0);
  kstep(X0, sp + tt); kstep(X1, sp + 8 + tt); kstep(X2, sp + 16 + tt); kstep(X3, sp + 24 + tt);
  SB();
  TRACE(127);
  float Ms = 0.f;
#pragma unroll 1
  for (int br = 0; br < 2; ++br) {
    const uint16_t* kb = br ? pk1 : pk0;
    const uint16_t* vb = br ? pv1 : pv0;
    {
      const __amdgpu_buffer_rsrc_t rc = mkr(kb, BUFB), rn = mkr(vb, BUFB);
      const int n0 = __builtin_amdgcn_readfirstlane(br ? 0 : 1 + YPRE);
#pragma unroll 1
      for (int n = n0; n < NB; ++n) {
        TRACE(br * 64 + n);
        if ((n & 7) == 0) { if (n) khead_end(br * 4 + (n >> 3) - 1); load_q(br * 4 + (n >> 3)); }
        float* dst = sp + n * 32 + tt;
        const unsigned o = uoff(n);
        const bool last = n == NB - 1;
        const __amdgpu_buffer_rsrc_t rl = last ? rn : rc;
        const unsigned on = uoff(last ? 0 : n + 1);
        bool deep, slow;
        pace_body(br * 64 + n, deep, slow);
        KHALF(S0, S2, rc, o + HS2, dst)
        KHALF(S1, S3, rc, o + HS3, dst + 8)
        KHALF(S2, S0, rl, on, dst + 16)
        KHALF(S3, S1, rl, on + HS1, dst + 24)
      }
      khead_end(br * 4 + 3);
    }
    if (br == 0) { STAMP(3); } else { STAMP(5); }
    {
      const __amdgpu_buffer_rsrc_t rc = mkr(vb, BUFB), rn = mkr(pk1, BUFB);
      const int nend = br ? NB - 1 : NB;
#pragma unroll 1
      for (int n = 0; n < nend; ++n) {
        TRACE(br * 64 + 32 + n);
        if ((n & 7) == 0) { if (n) vflush(br * 4 + (n >> 3) - 1); Ms = sm[((br * 4 + (n >> 3)) * NWAVES + wave) * PREC]; }
        float p0, p1, p2, p3;
        probs(n, Ms, p0, p1, p2, p3);
        const bool last = n == NB - 1;
        const __amdgpu_buffer_rsrc_t rl = last ? rn : rc;
        const unsigned o = uoff(n), on = uoff(last ? 0 : n + 1);
        bool deep, slow;
        pace_body(br * 64 + 32 + n, deep, slow);
        VHALF(S0, S2, rc, o + HS2, p0)
        VHALF(S1, S3, rc, o + HS3, p1)
        VHALF(S2, S0, rl, on, p2)
        VHALF(S3, S1, rl, on + HS1, p3)
      }
      if (br == 0) { vflush(3); STAMP(4); }
    }
  }
  {
    const __amdgpu_buffer_rsrc_t rc = mkr(pv1, BUFB);
    if (((NB - 1) & 7) == 0) { vflush(4 + ((NB - 1) >> 3) - 1); Ms = sm[((4 + ((NB - 1) >> 3)) * NWAVES + wave) * PREC]; }
    float p0, p1, p2, p3;
    probs(NB - 1, Ms, p0, p1, p2, p3);
    const unsigned o = uoff(NB - 1);
    LDS_(S2, rc, o + HS2); LDS_(S3, rc, o + HS3); SB();
    const __amdgpu_buffer_rsrc_t rwo = __builtin_amdgcn_make_buffer_rsrc((void*)((tbr ? po1 : po0) + (size_t)trow * D), 0, 12 * D * 2, 0x00020000);
#pragma unroll
    for (int r = 0; r < 12; ++r) {
      wa[r] = __builtin_amdgcn_raw_buffer_load_b128(rwo, lane * 20, r * D * 2, KVAUX);
      wb[r] = __builtin_amdgcn_raw_buffer_load_b32(rwo, lane * 20 + 16, r * D * 2, KVAUX);
    }
    SB(); vstep(S0, p0); PIN_ACC(); SB();
    vstep(S1, p1); PIN_ACC(); SB();
    vstep(S2, p2); PIN_ACC(); SB();
    vstep(S3, p3); PIN_ACC(); SB();
    vflush(7);
  }
  STAMP(6);
  __syncthreads();
  STAMP(7);

  if (EARLYPUB) {
    if (tid < 128) __builtin_amdgcn_raw_buffer_store_b128(merged_record(sm + 7 * NWAVES * PREC, tid, ta, tb), rws, WS_H1 + (g * WPG + j) * 16384 + 7 * 2048 + tid * 16, 0, AUX_SC1);
  } else {
#pragma unroll
    for (int i = 0; i < 4; ++i) { const int R = tid + 256 * i; __builtin_amdgcn_raw_buffer_store_b128(merged_record(sm + (R >> 7) * NWAVES * PREC, R & 127, ta, tb), rws, WS_H1 + (g * WPG + j) * 16384 + R * 16, 0, AUX_SC1); }
  }
  unsigned o6[3][2];
  {
    const int jj = lane & 31, half = lane >> 5;
    const int off = WS_H1 + (g * WPG + jj) * 16384 + ths * 2048 + ((j & 3) * 4 + wave) * 128 + half * 64;
    u32x4 r[4];
    sweep4(r, rws, off, ta, tb);
    const float mj = __uint_as_float(half ? r[3][0] : r[0][0]), lj = __uint_as_float(half ? r[3][2] : r[0][2]);
    const float M = max32(mj);
    const float wj = __builtin_amdgcn_exp2f(mj - M);
    const float inv = 1.f / sum32(wj * lj);
#pragma unroll
    for (int i = 0; i < 3; ++i) {
      const u32x4 q = half ? r[i] : r[i + 1];
      o6[i][0] = f2bf(sum32(wj * __uint_as_float(q[0])) * inv);
      o6[i][1] = f2bf(sum32(wj * __uint_as_float(q[2])) * inv);
    }
  }
  STAMP(8);
  {
    float y[10] = {0, 0, 0, 0, 0, 0, 0, 0, 0, 0};
#pragma unroll
    for (int r = 0; r < 12; ++r) {
      const unsigned olo = (unsigned)__builtin_amdgcn_readlane((int)o6[(r % 6) >> 1][r & 1], r < 6 ? 0 : 32), ohi = olo << 16;
#pragma unroll
      for (int e = 0; e < 4; ++e) { y[2 * e] = dot2(wa[r][e], olo, y[2 * e]); y[2 * e + 1] = dot2(wa[r][e], ohi, y[2 * e + 1]); }
      y[8] = dot2(wb[r], olo, y[8]); y[9] = dot2(wb[r], ohi, y[9]);
    }
#pragma unroll
    for (int e = 0; e < 5; ++e) *(float2*)&sred[wave * D + lane * 10 + 2 * e] = make_float2(y[2 * e], y[2 * e + 1]);
  }
  __syncthreads();
#pragma unroll
  for (int i = 0; i < 2; ++i) {
    const int n = tid + 256 * i;
    if (n < 320) {
      const float a = (sred[2 * n] + sred[D + 2 * n]) + (sred[2 * D + 2 * n] + sred[3 * D + 2 * n]);
      const float b = (sred[2 * n + 1] + sred[D + 2 * n + 1]) + (sred[2 * D + 2 * n + 1] + sred[3 * D + 2 * n + 1]);
      __builtin_amdgcn_raw_buffer_store_b128(mkrec(__float_as_uint(a), __float_as_uint(b), ta, tb), rws, WS_H2 + (tbr * 128 + g * 16 + (j & 15)) * 5120 + n * 16, 0, AUX_SC1);
    }
  }
  STAMP(9);
  if ((j & 15) < 5) {
    const int c = g * 5 + (j & 15);
    const int pp = wave * 32 + (lane & 31), half = lane >> 5;
    const int off = WS_H2 + (tbr * 128 + pp) * 5120 + (8 * c + 4 * half) * 16;
    u32x4 r[4];
    sweep4(r, rws, off, ta, tb);
#pragma unroll
    for (int i = 0; i < 4; ++i) {
      const float s0 = sum32(__uint_as_float(r[i][0])), s1 = sum32(__uint_as_float(r[i][2]));
      if ((lane & 31) == 0) { fin[wave * 16 + half * 8 + 2 * i] = s0; fin[wave * 16 + half * 8 + 2 * i + 1] = s1; }
    }
    __syncthreads();
    if (tid < 8) {
      const float a = (fin[2 * tid] + fin[16 + 2 * tid]) + (fin[32 + 2 * tid] + fin[48 + 2 * tid]);
      const float cc = (fin[2 * tid + 1] + fin[16 + 2 * tid + 1]) + (fin[32 + 2 * tid + 1] + fin[48 + 2 * tid + 1]);
      ((GAS unsigned*)pout)[(tbr * D + 16 * c) / 2 + tid] = f2bf(a) | (f2bf(cc) << 16);
    }
    if (bid == 0 && tid == 0) __hip_atomic_store((GAS unsigned long long*)(pws + WS_EPOCH), epoch + 1ull, __ATOMIC_RELAXED, __HIP_MEMORY_SCOPE_AGENT);
  }
}

extern "C" void kernel_launch(void* const* d_in, const int* in_sizes, int n_in, void* d_out, int out_size, void* d_ws, size_t ws_size, hipStream_t stream) {
  Params p;
  for (int b = 0; b < 2; ++b) {
    p.x[b] = (const uint16_t*)d_in[5 * b + 0];
    p.k[b] = (const uint16_t*)d_in[5 * b + 1];
    p.v[b] = (const uint16_t*)d_in[5 * b + 2];
    p.wq[b] = (const uint16_t*)d_in[5 * b + 3];
    p.wo[b] = (const uint16_t*)d_in[5 * b + 4];
  }
  p.out = (uint16_t*)d_out;
  p.ws = (unsigned char*)d_ws;
  (void)hipFuncSetAttribute((const void*)k_fused, hipFuncAttributeMaxDynamicSharedMemorySize, LDS_BYTES);
  k_fused<<<NWG, NTHR, LDS_BYTES, stream>>>(p);
}
```
